```python
import torch

def _softmax(x, axis=-1):
    return torch.softmax(x, dim=axis)
def _gelu(x):
    return torch.nn.functional.gelu(x, approximate="tanh")
def _silu(x):
    return torch.nn.functional.silu(x)
def _sigmoid(x):
    return torch.sigmoid(x)
def _layernorm(x, axis=-1, eps=1e-5):
    m = x.mean(dim=axis, keepdim=True)
    v = x.var(dim=axis, keepdim=True, unbiased=False)
    return (x - m) / torch.sqrt(v + eps)
def _scatter_add(tgt, idx, src, axis=0):
    return tgt.clone().index_add_(axis, idx, src)
def _segment_sum(src, seg, n):
    out = torch.zeros((n,) + src.shape[1:], dtype=src.dtype)
    return out.index_add_(0, seg, src)
def _pad(x, pw):
    flat = tuple(v for pair in reversed(pw) for v in pair)
    return torch.nn.functional.pad(x, flat)

kernel_name = 'standalone__gqa_block_cached__near_ridge'

def setup_inputs():

    _w = (lambda a: (lambda t: t.to(torch.bfloat16) if t.is_floating_point() else t)(torch.as_tensor(a)))
    B, Sq, D = 8, 256, 512
    Hq, Hkv, Sk, Dh = 32, 32, 1024, 128
    assert Hq % Hkv == 0, (Hq, Hkv)
    return {
        "x": _w((lambda *s: torch.randn(*s, dtype=torch.bfloat16) * 0.8)(B, Sq, D)),
        "k_cache": _w((lambda *s: torch.randn(*s, dtype=torch.bfloat16) * 0.8)(B, Hkv, Sk, Dh)),
        "v_cache": _w((lambda *s: torch.randn(*s, dtype=torch.bfloat16) * 0.05)(B, Hkv, Sk, Dh)),
        "wq": _w((lambda *s: torch.randn(*s, dtype=torch.bfloat16) * 0.8)(D, Hq * Dh) * (D ** -0.5)),
        "wo": _w((lambda *s: torch.randn(*s, dtype=torch.bfloat16) * 0.05)(Hq * Dh, D) * ((Hq * Dh) ** -0.5)),
    }

def reference(x, k_cache, v_cache, wq, wo):
    B, Sq, D = x.shape
    Hq, Dh = 32, 128
    Hkv = k_cache.shape[1]
    rep = Hq // Hkv
    q = (lambda x, *d: x.permute(*d))((x @ wq).reshape(B, Sq, Hq, Dh), 0, 2, 1, 3)
    k = (lambda x, n, axis: torch.repeat_interleave(x, n, dim=axis))(k_cache, rep, axis=1)
    v = (lambda x, n, axis: torch.repeat_interleave(x, n, dim=axis))(v_cache, rep, axis=1)
    scores = torch.einsum("bhqd,bhkd->bhqk", q, k) * (Dh ** -0.5)
    p = _softmax(scores, axis=-1)
    o = torch.einsum("bhqk,bhkd->bhqd", p, v)
    return (lambda x, *d: x.permute(*d))(o, 0, 2, 1, 3).reshape(B, Sq, Hq * Dh) @ wo
```

```cpp
#include <hip/hip_runtime.h>
#include <utility>

constexpr int NB = 8, SQ = 256, DM = 512, NH = 32, SK = 1024, DH = 128, NT = SK / 64;
constexpr float THR = 8.0f;
constexpr size_t SHM_ATTN = 163840;
typedef float    f32x16 __attribute__((ext_vector_type(16)));
typedef float    f32x2  __attribute__((ext_vector_type(2)));
typedef float    f32x4  __attribute__((ext_vector_type(4)));
typedef __bf16   bf16x8 __attribute__((ext_vector_type(8)));
typedef __bf16   bf16x4 __attribute__((ext_vector_type(4)));
typedef __bf16   bf16x2 __attribute__((ext_vector_type(2)));
typedef unsigned u32x4  __attribute__((ext_vector_type(4)));
typedef unsigned u32x2  __attribute__((ext_vector_type(2)));
template <int... Is> using iseq = std::integer_sequence<int, Is...>;
template <int N> using make_iseq = std::make_integer_sequence<int, N>;

#define SLOTB 0x8000u
#define VOFF  0x4000u
#define QBUF(i) (65536u + 49152u * ((i) & 1))
#define QWOFF 32768u
#define SRD_WORD3 0x00020000
#define DI __device__ __forceinline__
#define GAP() __builtin_amdgcn_sched_barrier(0)
#define W4_ACLOB "a0","a1","a2","a3","a4","a5","a6","a7","a8","a9","a10","a11","a12","a13","a14","a15","a16","a17","a18","a19","a20","a21","a22","a23","a24","a25","a26","a27","a28","a29","a30","a31","a32","a33","a34","a35","a36","a37","a38","a39","a40","a41","a42","a43","a44","a45","a46","a47","a48","a49","a50","a51","a52","a53","a54","a55","a56","a57","a58","a59","a60","a61","a62","a63","a64","a65","a66","a67","a68","a69","a70","a71","a72","a73","a74","a75","a76","a77","a78","a79","a80","a81","a82","a83","a84","a85","a86","a87","a88","a89","a90","a91","a92","a93","a94","a95","a96","a97","a98","a99","a100","a101","a102","a103","a104","a105","a106","a107","a108","a109","a110","a111","a112","a113","a114","a115","a116","a117","a118","a119","a120","a121","a122","a123","a124","a125","a126","a127"
#define W4_ACLOB_HI "a128","a129","a130","a131","a132","a133","a134","a135","a136","a137","a138","a139","a140","a141","a142","a143","a144","a145","a146","a147","a148","a149","a150","a151","a152","a153","a154","a155","a156","a157","a158","a159","a160","a161","a162","a163","a164","a165","a166","a167","a168","a169","a170","a171","a172","a173","a174","a175","a176","a177","a178","a179","a180","a181","a182","a183","a184","a185","a186","a187","a188","a189","a190","a191","a192","a193","a194","a195","a196","a197","a198","a199","a200","a201","a202","a203","a204","a205","a206","a207","a208","a209","a210","a211","a212","a213","a214","a215","a216","a217","a218","a219","a220","a221","a222","a223","a224","a225","a226","a227","a228","a229","a230","a231","a232","a233","a234","a235","a236","a237","a238","a239","a240","a241","a242","a243","a244","a245","a246","a247","a248","a249","a250","a251","a252","a253","a254","a255"
#define W4_RESCALE_TEXT "v_accvgpr_read_b32 %0, a0\n\tv_mul_f32 %0, %2, %0\n\tv_accvgpr_write_b32 a0, %0\n\t" "v_accvgpr_read_b32 %1, a1\n\tv_mul_f32 %1, %2, %1\n\tv_accvgpr_write_b32 a1, %1\n\t" "v_accvgpr_read_b32 %0, a2\n\tv_mul_f32 %0, %2, %0\n\tv_accvgpr_write_b32 a2, %0\n\t" "v_accvgpr_read_b32 %1, a3\n\tv_mul_f32 %1, %2, %1\n\tv_accvgpr_write_b32 a3, %1\n\t" "v_accvgpr_read_b32 %0, a4\n\tv_mul_f32 %0, %2, %0\n\tv_accvgpr_write_b32 a4, %0\n\t" "v_accvgpr_read_b32 %1, a5\n\tv_mul_f32 %1, %2, %1\n\tv_accvgpr_write_b32 a5, %1\n\t" "v_accvgpr_read_b32 %0, a6\n\tv_mul_f32 %0, %2, %0\n\tv_accvgpr_write_b32 a6, %0\n\t" "v_accvgpr_read_b32 %1, a7\n\tv_mul_f32 %1, %2, %1\n\tv_accvgpr_write_b32 a7, %1\n\t" "v_accvgpr_read_b32 %0, a8\n\tv_mul_f32 %0, %2, %0\n\tv_accvgpr_write_b32 a8, %0\n\t" "v_accvgpr_read_b32 %1, a9\n\tv_mul_f32 %1, %2, %1\n\tv_accvgpr_write_b32 a9, %1\n\t" "v_accvgpr_read_b32 %0, a10\n\tv_mul_f32 %0, %2, %0\n\tv_accvgpr_write_b32 a10, %0\n\t" "v_accvgpr_read_b32 %1, a11\n\tv_mul_f32 %1, %2, %1\n\tv_accvgpr_write_b32 a11, %1\n\t" "v_accvgpr_read_b32 %0, a12\n\tv_mul_f32 %0, %2, %0\n\tv_accvgpr_write_b32 a12, %0\n\t" "v_accvgpr_read_b32 %1, a13\n\tv_mul_f32 %1, %2, %1\n\tv_accvgpr_write_b32 a13, %1\n\t" "v_accvgpr_read_b32 %0, a14\n\tv_mul_f32 %0, %2, %0\n\tv_accvgpr_write_b32 a14, %0\n\t" "v_accvgpr_read_b32 %1, a15\n\tv_mul_f32 %1, %2, %1\n\tv_accvgpr_write_b32 a15, %1\n\t" "v_accvgpr_read_b32 %0, a16\n\tv_mul_f32 %0, %3, %0\n\tv_accvgpr_write_b32 a16, %0\n\t" "v_accvgpr_read_b32 %1, a17\n\tv_mul_f32 %1, %3, %1\n\tv_accvgpr_write_b32 a17, %1\n\t" "v_accvgpr_read_b32 %0, a18\n\tv_mul_f32 %0, %3, %0\n\tv_accvgpr_write_b32 a18, %0\n\t" "v_accvgpr_read_b32 %1, a19\n\tv_mul_f32 %1, %3, %1\n\tv_accvgpr_write_b32 a19, %1\n\t" "v_accvgpr_read_b32 %0, a20\n\tv_mul_f32 %0, %3, %0\n\tv_accvgpr_write_b32 a20, %0\n\t" "v_accvgpr_read_b32 %1, a21\n\tv_mul_f32 %1, %3, %1\n\tv_accvgpr_write_b32 a21, %1\n\t" "v_accvgpr_read_b32 %0, a22\n\tv_mul_f32 %0, %3, %0\n\tv_accvgpr_write_b32 a22, %0\n\t" "v_accvgpr_read_b32 %1, a23\n\tv_mul_f32 %1, %3, %1\n\tv_accvgpr_write_b32 a23, %1\n\t" "v_accvgpr_read_b32 %0, a24\n\tv_mul_f32 %0, %3, %0\n\tv_accvgpr_write_b32 a24, %0\n\t" "v_accvgpr_read_b32 %1, a25\n\tv_mul_f32 %1, %3, %1\n\tv_accvgpr_write_b32 a25, %1\n\t" "v_accvgpr_read_b32 %0, a26\n\tv_mul_f32 %0, %3, %0\n\tv_accvgpr_write_b32 a26, %0\n\t" "v_accvgpr_read_b32 %1, a27\n\tv_mul_f32 %1, %3, %1\n\tv_accvgpr_write_b32 a27, %1\n\t" "v_accvgpr_read_b32 %0, a28\n\tv_mul_f32 %0, %3, %0\n\tv_accvgpr_write_b32 a28, %0\n\t" "v_accvgpr_read_b32 %1, a29\n\tv_mul_f32 %1, %3, %1\n\tv_accvgpr_write_b32 a29, %1\n\t" "v_accvgpr_read_b32 %0, a30\n\tv_mul_f32 %0, %3, %0\n\tv_accvgpr_write_b32 a30, %0\n\t" "v_accvgpr_read_b32 %1, a31\n\tv_mul_f32 %1, %3, %1\n\tv_accvgpr_write_b32 a31, %1\n\t" "v_accvgpr_read_b32 %0, a32\n\tv_mul_f32 %0, %2, %0\n\tv_accvgpr_write_b32 a32, %0\n\t" "v_accvgpr_read_b32 %1, a33\n\tv_mul_f32 %1, %2, %1\n\tv_accvgpr_write_b32 a33, %1\n\t" "v_accvgpr_read_b32 %0, a34\n\tv_mul_f32 %0, %2, %0\n\tv_accvgpr_write_b32 a34, %0\n\t" "v_accvgpr_read_b32 %1, a35\n\tv_mul_f32 %1, %2, %1\n\tv_accvgpr_write_b32 a35, %1\n\t" "v_accvgpr_read_b32 %0, a36\n\tv_mul_f32 %0, %2, %0\n\tv_accvgpr_write_b32 a36, %0\n\t" "v_accvgpr_read_b32 %1, a37\n\tv_mul_f32 %1, %2, %1\n\tv_accvgpr_write_b32 a37, %1\n\t" "v_accvgpr_read_b32 %0, a38\n\tv_mul_f32 %0, %2, %0\n\tv_accvgpr_write_b32 a38, %0\n\t" "v_accvgpr_read_b32 %1, a39\n\tv_mul_f32 %1, %2, %1\n\tv_accvgpr_write_b32 a39, %1\n\t" "v_accvgpr_read_b32 %0, a40\n\tv_mul_f32 %0, %2, %0\n\tv_accvgpr_write_b32 a40, %0\n\t" "v_accvgpr_read_b32 %1, a41\n\tv_mul_f32 %1, %2, %1\n\tv_accvgpr_write_b32 a41, %1\n\t" "v_accvgpr_read_b32 %0, a42\n\tv_mul_f32 %0, %2, %0\n\tv_accvgpr_write_b32 a42, %0\n\t" "v_accvgpr_read_b32 %1, a43\n\tv_mul_f32 %1, %2, %1\n\tv_accvgpr_write_b32 a43, %1\n\t" "v_accvgpr_read_b32 %0, a44\n\tv_mul_f32 %0, %2, %0\n\tv_accvgpr_write_b32 a44, %0\n\t" "v_accvgpr_read_b32 %1, a45\n\tv_mul_f32 %1, %2, %1\n\tv_accvgpr_write_b32 a45, %1\n\t" "v_accvgpr_read_b32 %0, a46\n\tv_mul_f32 %0, %2, %0\n\tv_accvgpr_write_b32 a46, %0\n\t" "v_accvgpr_read_b32 %1, a47\n\tv_mul_f32 %1, %2, %1\n\tv_accvgpr_write_b32 a47, %1\n\t" "v_accvgpr_read_b32 %0, a48\n\tv_mul_f32 %0, %3, %0\n\tv_accvgpr_write_b32 a48, %0\n\t" "v_accvgpr_read_b32 %1, a49\n\tv_mul_f32 %1, %3, %1\n\tv_accvgpr_write_b32 a49, %1\n\t" "v_accvgpr_read_b32 %0, a50\n\tv_mul_f32 %0, %3, %0\n\tv_accvgpr_write_b32 a50, %0\n\t" "v_accvgpr_read_b32 %1, a51\n\tv_mul_f32 %1, %3, %1\n\tv_accvgpr_write_b32 a51, %1\n\t" "v_accvgpr_read_b32 %0, a52\n\tv_mul_f32 %0, %3, %0\n\tv_accvgpr_write_b32 a52, %0\n\t" "v_accvgpr_read_b32 %1, a53\n\tv_mul_f32 %1, %3, %1\n\tv_accvgpr_write_b32 a53, %1\n\t" "v_accvgpr_read_b32 %0, a54\n\tv_mul_f32 %0, %3, %0\n\tv_accvgpr_write_b32 a54, %0\n\t" "v_accvgpr_read_b32 %1, a55\n\tv_mul_f32 %1, %3, %1\n\tv_accvgpr_write_b32 a55, %1\n\t" "v_accvgpr_read_b32 %0, a56\n\tv_mul_f32 %0, %3, %0\n\tv_accvgpr_write_b32 a56, %0\n\t" "v_accvgpr_read_b32 %1, a57\n\tv_mul_f32 %1, %3, %1\n\tv_accvgpr_write_b32 a57, %1\n\t" "v_accvgpr_read_b32 %0, a58\n\tv_mul_f32 %0, %3, %0\n\tv_accvgpr_write_b32 a58, %0\n\t" "v_accvgpr_read_b32 %1, a59\n\tv_mul_f32 %1, %3, %1\n\tv_accvgpr_write_b32 a59, %1\n\t" "v_accvgpr_read_b32 %0, a60\n\tv_mul_f32 %0, %3, %0\n\tv_accvgpr_write_b32 a60, %0\n\t" "v_accvgpr_read_b32 %1, a61\n\tv_mul_f32 %1, %3, %1\n\tv_accvgpr_write_b32 a61, %1\n\t" "v_accvgpr_read_b32 %0, a62\n\tv_mul_f32 %0, %3, %0\n\tv_accvgpr_write_b32 a62, %0\n\t" "v_accvgpr_read_b32 %1, a63\n\tv_mul_f32 %1, %3, %1\n\tv_accvgpr_write_b32 a63, %1\n\t" "v_accvgpr_read_b32 %0, a64\n\tv_mul_f32 %0, %2, %0\n\tv_accvgpr_write_b32 a64, %0\n\t" "v_accvgpr_read_b32 %1, a65\n\tv_mul_f32 %1, %2, %1\n\tv_accvgpr_write_b32 a65, %1\n\t" "v_accvgpr_read_b32 %0, a66\n\tv_mul_f32 %0, %2, %0\n\tv_accvgpr_write_b32 a66, %0\n\t" "v_accvgpr_read_b32 %1, a67\n\tv_mul_f32 %1, %2, %1\n\tv_accvgpr_write_b32 a67, %1\n\t" "v_accvgpr_read_b32 %0, a68\n\tv_mul_f32 %0, %2, %0\n\tv_accvgpr_write_b32 a68, %0\n\t" "v_accvgpr_read_b32 %1, a69\n\tv_mul_f32 %1, %2, %1\n\tv_accvgpr_write_b32 a69, %1\n\t" "v_accvgpr_read_b32 %0, a70\n\tv_mul_f32 %0, %2, %0\n\tv_accvgpr_write_b32 a70, %0\n\t" "v_accvgpr_read_b32 %1, a71\n\tv_mul_f32 %1, %2, %1\n\tv_accvgpr_write_b32 a71, %1\n\t" "v_accvgpr_read_b32 %0, a72\n\tv_mul_f32 %0, %2, %0\n\tv_accvgpr_write_b32 a72, %0\n\t" "v_accvgpr_read_b32 %1, a73\n\tv_mul_f32 %1, %2, %1\n\tv_accvgpr_write_b32 a73, %1\n\t" "v_accvgpr_read_b32 %0, a74\n\tv_mul_f32 %0, %2, %0\n\tv_accvgpr_write_b32 a74, %0\n\t" "v_accvgpr_read_b32 %1, a75\n\tv_mul_f32 %1, %2, %1\n\tv_accvgpr_write_b32 a75, %1\n\t" "v_accvgpr_read_b32 %0, a76\n\tv_mul_f32 %0, %2, %0\n\tv_accvgpr_write_b32 a76, %0\n\t" "v_accvgpr_read_b32 %1, a77\n\tv_mul_f32 %1, %2, %1\n\tv_accvgpr_write_b32 a77, %1\n\t" "v_accvgpr_read_b32 %0, a78\n\tv_mul_f32 %0, %2, %0\n\tv_accvgpr_write_b32 a78, %0\n\t" "v_accvgpr_read_b32 %1, a79\n\tv_mul_f32 %1, %2, %1\n\tv_accvgpr_write_b32 a79, %1\n\t" "v_accvgpr_read_b32 %0, a80\n\tv_mul_f32 %0, %3, %0\n\tv_accvgpr_write_b32 a80, %0\n\t" "v_accvgpr_read_b32 %1, a81\n\tv_mul_f32 %1, %3, %1\n\tv_accvgpr_write_b32 a81, %1\n\t" "v_accvgpr_read_b32 %0, a82\n\tv_mul_f32 %0, %3, %0\n\tv_accvgpr_write_b32 a82, %0\n\t" "v_accvgpr_read_b32 %1, a83\n\tv_mul_f32 %1, %3, %1\n\tv_accvgpr_write_b32 a83, %1\n\t" "v_accvgpr_read_b32 %0, a84\n\tv_mul_f32 %0, %3, %0\n\tv_accvgpr_write_b32 a84, %0\n\t" "v_accvgpr_read_b32 %1, a85\n\tv_mul_f32 %1, %3, %1\n\tv_accvgpr_write_b32 a85, %1\n\t" "v_accvgpr_read_b32 %0, a86\n\tv_mul_f32 %0, %3, %0\n\tv_accvgpr_write_b32 a86, %0\n\t" "v_accvgpr_read_b32 %1, a87\n\tv_mul_f32 %1, %3, %1\n\tv_accvgpr_write_b32 a87, %1\n\t" "v_accvgpr_read_b32 %0, a88\n\tv_mul_f32 %0, %3, %0\n\tv_accvgpr_write_b32 a88, %0\n\t" "v_accvgpr_read_b32 %1, a89\n\tv_mul_f32 %1, %3, %1\n\tv_accvgpr_write_b32 a89, %1\n\t" "v_accvgpr_read_b32 %0, a90\n\tv_mul_f32 %0, %3, %0\n\tv_accvgpr_write_b32 a90, %0\n\t" "v_accvgpr_read_b32 %1, a91\n\tv_mul_f32 %1, %3, %1\n\tv_accvgpr_write_b32 a91, %1\n\t" "v_accvgpr_read_b32 %0, a92\n\tv_mul_f32 %0, %3, %0\n\tv_accvgpr_write_b32 a92, %0\n\t" "v_accvgpr_read_b32 %1, a93\n\tv_mul_f32 %1, %3, %1\n\tv_accvgpr_write_b32 a93, %1\n\t" "v_accvgpr_read_b32 %0, a94\n\tv_mul_f32 %0, %3, %0\n\tv_accvgpr_write_b32 a94, %0\n\t" "v_accvgpr_read_b32 %1, a95\n\tv_mul_f32 %1, %3, %1\n\tv_accvgpr_write_b32 a95, %1\n\t" "v_accvgpr_read_b32 %0, a96\n\tv_mul_f32 %0, %2, %0\n\tv_accvgpr_write_b32 a96, %0\n\t" "v_accvgpr_read_b32 %1, a97\n\tv_mul_f32 %1, %2, %1\n\tv_accvgpr_write_b32 a97, %1\n\t" "v_accvgpr_read_b32 %0, a98\n\tv_mul_f32 %0, %2, %0\n\tv_accvgpr_write_b32 a98, %0\n\t" "v_accvgpr_read_b32 %1, a99\n\tv_mul_f32 %1, %2, %1\n\tv_accvgpr_write_b32 a99, %1\n\t" "v_accvgpr_read_b32 %0, a100\n\tv_mul_f32 %0, %2, %0\n\tv_accvgpr_write_b32 a100, %0\n\t" "v_accvgpr_read_b32 %1, a101\n\tv_mul_f32 %1, %2, %1\n\tv_accvgpr_write_b32 a101, %1\n\t" "v_accvgpr_read_b32 %0, a102\n\tv_mul_f32 %0, %2, %0\n\tv_accvgpr_write_b32 a102, %0\n\t" "v_accvgpr_read_b32 %1, a103\n\tv_mul_f32 %1, %2, %1\n\tv_accvgpr_write_b32 a103, %1\n\t" "v_accvgpr_read_b32 %0, a104\n\tv_mul_f32 %0, %2, %0\n\tv_accvgpr_write_b32 a104, %0\n\t" "v_accvgpr_read_b32 %1, a105\n\tv_mul_f32 %1, %2, %1\n\tv_accvgpr_write_b32 a105, %1\n\t" "v_accvgpr_read_b32 %0, a106\n\tv_mul_f32 %0, %2, %0\n\tv_accvgpr_write_b32 a106, %0\n\t" "v_accvgpr_read_b32 %1, a107\n\tv_mul_f32 %1, %2, %1\n\tv_accvgpr_write_b32 a107, %1\n\t" "v_accvgpr_read_b32 %0, a108\n\tv_mul_f32 %0, %2, %0\n\tv_accvgpr_write_b32 a108, %0\n\t" "v_accvgpr_read_b32 %1, a109\n\tv_mul_f32 %1, %2, %1\n\tv_accvgpr_write_b32 a109, %1\n\t" "v_accvgpr_read_b32 %0, a110\n\tv_mul_f32 %0, %2, %0\n\tv_accvgpr_write_b32 a110, %0\n\t" "v_accvgpr_read_b32 %1, a111\n\tv_mul_f32 %1, %2, %1\n\tv_accvgpr_write_b32 a111, %1\n\t" "v_accvgpr_read_b32 %0, a112\n\tv_mul_f32 %0, %3, %0\n\tv_accvgpr_write_b32 a112, %0\n\t" "v_accvgpr_read_b32 %1, a113\n\tv_mul_f32 %1, %3, %1\n\tv_accvgpr_write_b32 a113, %1\n\t" "v_accvgpr_read_b32 %0, a114\n\tv_mul_f32 %0, %3, %0\n\tv_accvgpr_write_b32 a114, %0\n\t" "v_accvgpr_read_b32 %1, a115\n\tv_mul_f32 %1, %3, %1\n\tv_accvgpr_write_b32 a115, %1\n\t" "v_accvgpr_read_b32 %0, a116\n\tv_mul_f32 %0, %3, %0\n\tv_accvgpr_write_b32 a116, %0\n\t" "v_accvgpr_read_b32 %1, a117\n\tv_mul_f32 %1, %3, %1\n\tv_accvgpr_write_b32 a117, %1\n\t" "v_accvgpr_read_b32 %0, a118\n\tv_mul_f32 %0, %3, %0\n\tv_accvgpr_write_b32 a118, %0\n\t" "v_accvgpr_read_b32 %1, a119\n\tv_mul_f32 %1, %3, %1\n\tv_accvgpr_write_b32 a119, %1\n\t" "v_accvgpr_read_b32 %0, a120\n\tv_mul_f32 %0, %3, %0\n\tv_accvgpr_write_b32 a120, %0\n\t" "v_accvgpr_read_b32 %1, a121\n\tv_mul_f32 %1, %3, %1\n\tv_accvgpr_write_b32 a121, %1\n\t" "v_accvgpr_read_b32 %0, a122\n\tv_mul_f32 %0, %3, %0\n\tv_accvgpr_write_b32 a122, %0\n\t" "v_accvgpr_read_b32 %1, a123\n\tv_mul_f32 %1, %3, %1\n\tv_accvgpr_write_b32 a123, %1\n\t" "v_accvgpr_read_b32 %0, a124\n\tv_mul_f32 %0, %3, %0\n\tv_accvgpr_write_b32 a124, %0\n\t" "v_accvgpr_read_b32 %1, a125\n\tv_mul_f32 %1, %3, %1\n\tv_accvgpr_write_b32 a125, %1\n\t" "v_accvgpr_read_b32 %0, a126\n\tv_mul_f32 %0, %3, %0\n\tv_accvgpr_write_b32 a126, %0\n\t" "v_accvgpr_read_b32 %1, a127\n\tv_mul_f32 %1, %3, %1\n\tv_accvgpr_write_b32 a127, %1\n\t"


DI unsigned rfl(unsigned x) { return __builtin_amdgcn_readfirstlane(x); }
DI unsigned cvtpk(float lo, float hi) {
  f32x2 v = {lo, hi}; bf16x2 b = __builtin_convertvector(v, bf16x2); return __builtin_bit_cast(unsigned, b);
}
DI float max3f(float a, float b, float c) { float r; asm("v_max3_f32 %0, %1, %2, %3" : "=v"(r) : "v"(a), "v"(b), "v"(c)); return r; }
DI float max2f(float a, float b) { float r; asm("v_max_f32_e32 %0, %1, %2" : "=v"(r) : "v"(a), "v"(b)); return r; }
DI float fadd_s(float a, float b) { float r; asm("v_add_f32_e32 %0, %1, %2" : "=v"(r) : "v"(a), "v"(b)); return r; }
DI float fmul_s(float a, float b) { float r; asm("v_mul_f32_e32 %0, %1, %2" : "=v"(r) : "v"(a), "v"(b)); return r; }
DI float fsub_s(float a, float b) { float r; asm("v_sub_f32_e32 %0, %1, %2" : "=v"(r) : "v"(a), "v"(b)); return r; }
DI float w4_aone() { unsigned l; asm volatile("v_mbcnt_lo_u32_b32 %0, -1, 0\n\tv_mbcnt_hi_u32_b32 %0, -1, %0" : "=v"(l)); return l < 32u ? 1.f : 0.f; }
DI void negm_bcast(f32x16& negm, float aone, float neg_mhat) {
  asm volatile("s_nop 1\n\tv_mfma_f32_32x32x2_f32 %0, %1, %2, 0" : "+v"(negm) : "v"(aone), "v"(neg_mhat));
}

struct W4C {
  unsigned vkb[4], vvb, vksrc[2], vvsrc, m0k, m0v, sofk, sofv, ldsb;
  __amdgpu_buffer_rsrc_t srdK, srdV;
  f32x16 S[2][2][2];
  f32x16 NEGM[2];
  bf16x8 Vf[4][4];
  bf16x8 Pk[2][4];
  bf16x4 vlo[4][4], vup[4][4];
  float mhat[2], lsum[2], fct[2]; bool pend;
  float rs0[2], rs1[2], pmx0[2], pmx1[2], rmax[2];
  unsigned pm0[8], psf[8];
};

constexpr int AO_(int db, int qb) { return 16 * (2 * db + qb); }
constexpr int AQ_(int qb, int d0) { return 128 + 32 * qb + 4 * d0; }
constexpr int AK_(int kb, int d0) { return 192 + 32 * kb + 4 * d0; }

template <int I> DI void tr_read(W4C& c, unsigned vslot) {
  constexpr int ks = I >> 3, db = (I >> 1) & 3;
  if constexpr ((I & 1) == 0) asm volatile("ds_read_b64_tr_b16 %0, %1 offset:%2" : "=v"(c.vlo[db][ks]) : "v"(c.vvb + vslot), "i"(4096 * ks + 512 * db) : "memory");
  else                        asm volatile("ds_read_b64_tr_b16 %0, %1 offset:%2" : "=v"(c.vup[db][ks]) : "v"(c.vvb + vslot), "i"(4096 * ks + 2048 + 512 * db) : "memory");
}
template <int I> DI void k_read(W4C& c, unsigned slot) {
  constexpr int kb = I >> 3, d0 = I & 7;
  asm volatile("ds_read_b128 a[%c1:%c2], %0 offset:%c3" :: "v"(c.vkb[d0 & 3] + slot), "i"(AK_(kb, d0)), "i"(AK_(kb, d0) + 3), "i"(8192 * kb + 128 * (d0 >> 2)) : "memory", W4_ACLOB_HI);
}
template <int X, bool FIRST, int M> DI void qk_mfma(W4C& c) {
  constexpr int d0 = M >> 2, kb = (M >> 1) & 1, qb = M & 1, ak = AK_(kb, d0), aq = AQ_(qb, d0);
  if constexpr (d0 == 0) {
    if constexpr (FIRST) asm volatile("v_mfma_f32_32x32x16_bf16 %0, a[%c1:%c2], a[%c3:%c4], 0" : "=&v"(c.S[X][kb][qb]) : "i"(ak), "i"(ak + 3), "i"(aq), "i"(aq + 3) : W4_ACLOB, W4_ACLOB_HI);
    else                 asm volatile("v_mfma_f32_32x32x16_bf16 %0, a[%c2:%c3], a[%c4:%c5], %1" : "=&v"(c.S[X][kb][qb]) : "v"(c.NEGM[qb]), "i"(ak), "i"(ak + 3), "i"(aq), "i"(aq + 3) : W4_ACLOB, W4_ACLOB_HI);
  } else asm volatile("v_mfma_f32_32x32x16_bf16 %0, a[%c1:%c2], a[%c3:%c4], %0" : "+v"(c.S[X][kb][qb]) : "i"(ak), "i"(ak + 3), "i"(aq), "i"(aq + 3) : W4_ACLOB, W4_ACLOB_HI);
}
template <int M, bool ZERO> DI void pv_mfma(W4C& c) {
  constexpr int ks = M >> 3, db = (M >> 1) & 3, qb = M & 1, ao = AO_(db, qb);
  if constexpr (ZERO && ks == 0) asm volatile("v_mfma_f32_32x32x16_bf16 a[%c2:%c3], %0, %1, 0" :: "v"(c.Vf[db][ks]), "v"(c.Pk[qb][ks]), "i"(ao), "i"(ao + 15) : W4_ACLOB);
  else                         asm volatile("v_mfma_f32_32x32x16_bf16 a[%c2:%c3], %0, %1, a[%c2:%c3]" :: "v"(c.Vf[db][ks]), "v"(c.Pk[qb][ks]), "i"(ao), "i"(ao + 15) : W4_ACLOB);
}
template <int X, int QB, int KS, int P> DI void pack1(W4C& c) {
  u32x4 w = __builtin_bit_cast(u32x4, c.Pk[QB][KS]);
  w[P] = cvtpk(c.S[X][KS >> 1][QB][8 * (KS & 1) + 2 * P], c.S[X][KS >> 1][QB][8 * (KS & 1) + 2 * P + 1]);
  c.Pk[QB][KS] = __builtin_bit_cast(bf16x8, w);
}
template <int X, int J> DI void rowsum1(W4C& c) {
  constexpr int qb = J >> 4, ks = (J >> 2) & 3, p = J & 3;
  c.rs0[qb] = fadd_s(c.rs0[qb], c.S[X][ks >> 1][qb][8 * (ks & 1) + 2 * p]); c.rs1[qb] = fadd_s(c.rs1[qb], c.S[X][ks >> 1][qb][8 * (ks & 1) + 2 * p + 1]);
}

template <int X, bool FIRST, bool DMA, int M> DI void gapA(W4C& c, unsigned vslot, const int t) {
  constexpr int Xp = X ^ 1;
  qk_mfma<X, FIRST, M>(c);
  if constexpr (DMA && M >= 16 && M <= 31) {
    const unsigned tn = (unsigned)(t + 3), sl = (tn & 3u) * SLOTB;
    constexpr int q = (M - 16) >> 1;
    if constexpr (((M - 16) & 1) == 0) {
      if constexpr (q < 4) c.pm0[q] = c.m0k + sl + 1024u * q; else c.pm0[q] = c.m0v + sl + 1024u * (q - 4);
      asm volatile("" : "+s"(c.pm0[q]));
    } else {
      if constexpr (q < 4) c.psf[q] = 16384u * tn + c.sofk + 1024u * q; else c.psf[q] = 16384u * tn + c.sofv + 2048u * ((q - 4) >> 1) + 128u * ((q - 4) & 1);
      asm volatile("" : "+s"(c.psf[q]));
    }
  }
  if constexpr (DMA && M == 31) asm volatile("s_nop 4" : "+s"(c.pm0[0]), "+s"(c.pm0[1]), "+s"(c.pm0[2]), "+s"(c.pm0[3]), "+s"(c.pm0[4]), "+s"(c.pm0[5]), "+s"(c.pm0[6]), "+s"(c.pm0[7]),
                                                  "+s"(c.psf[0]), "+s"(c.psf[1]), "+s"(c.psf[2]), "+s"(c.psf[3]), "+s"(c.psf[4]), "+s"(c.psf[5]), "+s"(c.psf[6]), "+s"(c.psf[7]));
  if constexpr (!FIRST && M == 8) asm volatile("" :: "v"(c.NEGM[0]), "v"(c.NEGM[1]));
  if constexpr (!FIRST) {
    constexpr int base = M + (M >= 4) + (M >= 8) + (M >= 12) + (M >= 16) + (M >= 20) + (M >= 24);
    if constexpr (M <= 25) tr_read<base>(c, vslot);
    if constexpr (M == 3 || M == 7 || M == 11 || M == 15 || M == 19 || M == 23) tr_read<base + 1>(c, vslot);
    if constexpr (M < 16) {
      constexpr int e0 = 2 * M, e1 = 2 * M + 1;
      c.S[Xp][1][e0 >> 4][e0 & 15] = __builtin_amdgcn_exp2f(c.S[Xp][1][e0 >> 4][e0 & 15]);
      c.S[Xp][1][e1 >> 4][e1 & 15] = __builtin_amdgcn_exp2f(c.S[Xp][1][e1 >> 4][e1 & 15]);
      pack1<Xp, (M >> 3), ((M >> 2) & 1), (M & 3)>(c);
    } else {
      constexpr int cc = M - 16;
      pack1<Xp, (cc >> 3), 2 + ((cc >> 2) & 1), (cc & 3)>(c);
      rowsum1<Xp, 16 * (cc >> 3) + 8 + 4 * ((cc >> 2) & 1) + (cc & 3)>(c);
    }
  }
  GAP();
}
#define W4_VLO "+v"(c.vlo[0][0]), "+v"(c.vlo[0][1]), "+v"(c.vlo[0][2]), "+v"(c.vlo[0][3]), "+v"(c.vlo[1][0]), "+v"(c.vlo[1][1]), "+v"(c.vlo[1][2]), "+v"(c.vlo[1][3]), \
               "+v"(c.vlo[2][0]), "+v"(c.vlo[2][1]), "+v"(c.vlo[2][2]), "+v"(c.vlo[2][3]), "+v"(c.vlo[3][0]), "+v"(c.vlo[3][1]), "+v"(c.vlo[3][2]), "+v"(c.vlo[3][3])
#define W4_VUP "+v"(c.vup[0][0]), "+v"(c.vup[0][1]), "+v"(c.vup[0][2]), "+v"(c.vup[0][3]), "+v"(c.vup[1][0]), "+v"(c.vup[1][1]), "+v"(c.vup[1][2]), "+v"(c.vup[1][3]), \
               "+v"(c.vup[2][0]), "+v"(c.vup[2][1]), "+v"(c.vup[2][2]), "+v"(c.vup[2][3]), "+v"(c.vup[3][0]), "+v"(c.vup[3][1]), "+v"(c.vup[3][2]), "+v"(c.vup[3][3])
template <int X, bool FIRST, bool DMA, int VM, int... Ms> DI void phaseA_(W4C& c, const int t, iseq<Ms...>) {
  const unsigned vslot = (unsigned)((t - 1) & 3) * SLOTB;
  (gapA<X, FIRST, DMA, Ms>(c, vslot, t), ...);
  if constexpr (!FIRST) {
    #pragma unroll
    for (int qb = 0; qb < 2; ++qb) {
      const float rs = fadd_s(c.rs0[qb], c.rs1[qb]);
      auto pr = __builtin_amdgcn_permlane32_swap(__float_as_uint(rs), __float_as_uint(rs), false, false);
      c.lsum[qb] = fadd_s(c.lsum[qb], fadd_s(__uint_as_float(pr[0]), __uint_as_float(pr[1])));
    }
  }
  if constexpr (FIRST) asm volatile("s_waitcnt vmcnt(%0) lgkmcnt(0)\n\ts_barrier" :: "i"(VM) : "memory");
  else { asm volatile("s_waitcnt vmcnt(%16) lgkmcnt(0)" : W4_VLO : "i"(VM) : "memory"); asm volatile("s_barrier" : W4_VUP :: "memory"); }
  GAP();
  if constexpr (!FIRST) {
    #pragma unroll
    for (int ks = 0; ks < 4; ++ks)
      #pragma unroll
      for (int db = 0; db < 4; ++db) c.Vf[db][ks] = __builtin_shufflevector(c.vlo[db][ks], c.vup[db][ks], 0, 1, 2, 3, 4, 5, 6, 7);
  }
}
template <int X, bool FIRST, bool DMA, int VM> DI void phaseA(W4C& c, const int t) { phaseA_<X, FIRST, DMA, VM>(c, t, make_iseq<32>{}); }

template <int X, bool PEEL, bool ZERO1, bool DMA, bool KR, int M> DI void gapB(W4C& c, const unsigned kslot_rd) {
  if constexpr (!PEEL) pv_mfma<M, ZERO1>(c);
  if constexpr (DMA && M < 4)            asm volatile("s_mov_b32 m0, %0\n\ts_nop 0\n\tbuffer_load_dwordx4 %1, %2, %3 offen lds" :: "s"(c.pm0[M]), "v"(c.vksrc[M & 1]), "s"(c.srdK),  "s"(c.psf[M]) : "m0", "memory");
  else if constexpr (DMA && M >= 4 && M < 8) asm volatile("s_mov_b32 m0, %0\n\ts_nop 0\n\tbuffer_load_dwordx4 %1, %2, %3 offen lds" :: "s"(c.pm0[M]), "v"(c.vvsrc), "s"(c.srdV),  "s"(c.psf[M]) : "m0", "memory");
  if constexpr (M >= 1 && M <= 16 && KR) k_read<M - 1>(c, kslot_rd);
  if constexpr (M >= 8 && M < 16) {
    constexpr int i = M - 8, q = i >> 2, r = 4 * (i & 3);
    if constexpr ((i & 3) == 0) { c.pmx0[q] = max3f(c.S[X][0][q][0], c.S[X][0][q][1], c.S[X][1][q][0]); c.pmx1[q] = max3f(c.S[X][0][q][2], c.S[X][0][q][3], c.S[X][1][q][1]);
                                  c.pmx0[q] = max3f(c.pmx0[q], c.S[X][1][q][2], c.S[X][1][q][3]); }
    else                        { c.pmx0[q] = max3f(c.pmx0[q], c.S[X][0][q][r], c.S[X][0][q][r + 1]); c.pmx1[q] = max3f(c.pmx1[q], c.S[X][0][q][r + 2], c.S[X][0][q][r + 3]);
                                  c.pmx0[q] = max3f(c.pmx0[q], c.S[X][1][q][r], c.S[X][1][q][r + 1]); c.pmx1[q] = max3f(c.pmx1[q], c.S[X][1][q][r + 2], c.S[X][1][q][r + 3]); }
  }
  if constexpr (M == 16 || M == 17) {
    constexpr int q = M - 16; const float mm = max2f(c.pmx0[q], c.pmx1[q]);
    auto pr = __builtin_amdgcn_permlane32_swap(__float_as_uint(mm), __float_as_uint(mm), false, false);
    c.rmax[q] = max2f(__uint_as_float(pr[0]), __uint_as_float(pr[1]));
  }
  if constexpr (M == 18) {
    if constexpr (PEEL) {
      #pragma unroll
      for (int q = 0; q < 2; ++q) { c.mhat[q] = c.rmax[q]; c.lsum[q] = 0.f; c.fct[q] = 1.f;
        #pragma unroll
        for (int kb = 0; kb < 2; ++kb) { _Pragma("unroll") for (int e = 0; e < 16; ++e) c.S[X][kb][q][e] = fsub_s(c.S[X][kb][q][e], c.rmax[q]); }
        negm_bcast(c.NEGM[q], w4_aone(), -c.mhat[q]);
      }
    } else {
      const bool grew = (c.rmax[0] > THR) | (c.rmax[1] > THR);
      if (__builtin_expect(__any((int)grew), 0)) {
        #pragma unroll
        for (int q = 0; q < 2; ++q) { const float dl = max2f(c.rmax[q], 0.f); c.mhat[q] = fadd_s(c.mhat[q], dl); c.fct[q] = __builtin_amdgcn_exp2f(-dl);
          #pragma unroll
          for (int kb = 0; kb < 2; ++kb) { _Pragma("unroll") for (int e = 0; e < 16; ++e) c.S[X][kb][q][e] = fsub_s(c.S[X][kb][q][e], dl); }
          negm_bcast(c.NEGM[q], w4_aone(), -c.mhat[q]);
        }
        c.pend = true;
      }
    }
  }
  if constexpr (M == 18) { c.rs0[0] = c.rs0[1] = c.rs1[0] = c.rs1[1] = 0.f; }
  if constexpr (M >= 19) {
    constexpr int g = M - 19, e_lo = (32 * g) / 13, e_hi = (32 * (g + 1)) / 13;
    {
      constexpr int p_lo = g ? (32 * (g - 1)) / 13 : 0, p_hi = g ? (32 * g) / 13 : 0;
      if constexpr (p_lo + 0 < p_hi) { constexpr int e = p_lo + 0; if constexpr (e & 1) c.rs1[e >> 4] = fadd_s(c.rs1[e >> 4], c.S[X][0][e >> 4][e & 15]); else c.rs0[e >> 4] = fadd_s(c.rs0[e >> 4], c.S[X][0][e >> 4][e & 15]); }
      if constexpr (p_lo + 1 < p_hi) { constexpr int e = p_lo + 1; if constexpr (e & 1) c.rs1[e >> 4] = fadd_s(c.rs1[e >> 4], c.S[X][0][e >> 4][e & 15]); else c.rs0[e >> 4] = fadd_s(c.rs0[e >> 4], c.S[X][0][e >> 4][e & 15]); }
      if constexpr (p_lo + 2 < p_hi) { constexpr int e = p_lo + 2; if constexpr (e & 1) c.rs1[e >> 4] = fadd_s(c.rs1[e >> 4], c.S[X][0][e >> 4][e & 15]); else c.rs0[e >> 4] = fadd_s(c.rs0[e >> 4], c.S[X][0][e >> 4][e & 15]); }
    }
    if constexpr (e_lo + 0 < e_hi) c.S[X][0][(e_lo + 0) >> 4][(e_lo + 0) & 15] = __builtin_amdgcn_exp2f(c.S[X][0][(e_lo + 0) >> 4][(e_lo + 0) & 15]);
    if constexpr (e_lo + 1 < e_hi) c.S[X][0][(e_lo + 1) >> 4][(e_lo + 1) & 15] = __builtin_amdgcn_exp2f(c.S[X][0][(e_lo + 1) >> 4][(e_lo + 1) & 15]);
    if constexpr (e_lo + 2 < e_hi) c.S[X][0][(e_lo + 2) >> 4][(e_lo + 2) & 15] = __builtin_amdgcn_exp2f(c.S[X][0][(e_lo + 2) >> 4][(e_lo + 2) & 15]);
  }
  if constexpr (M == 31) asm volatile("" :: "v"(c.S[X][0][0]), "v"(c.S[X][0][1]), "v"(c.NEGM[0]), "v"(c.NEGM[1]), "v"(c.rs0[0]), "v"(c.rs0[1]), "v"(c.rs1[0]), "v"(c.rs1[1]));
  GAP();
}
template <int X, bool PEEL, bool ZERO1, bool DMA, bool KR, int... Ms> DI void phaseB_(W4C& c, const int t, iseq<Ms...>) {
  const unsigned kslot_rd = (unsigned)((t + 1) & 3) * SLOTB;
  if constexpr (!PEEL) asm volatile("s_nop 1" ::: "memory");
  GAP();
  (gapB<X, PEEL, ZERO1, DMA, KR, Ms>(c, kslot_rd), ...);
  {
    #pragma unroll
    for (int e = (32 * 12) / 13; e < 32; ++e) { if (e & 1) c.rs1[e >> 4] = fadd_s(c.rs1[e >> 4], c.S[X][0][e >> 4][e & 15]); else c.rs0[e >> 4] = fadd_s(c.rs0[e >> 4], c.S[X][0][e >> 4][e & 15]); }
  }
  if constexpr (!PEEL) {
    if (__builtin_expect(c.pend, 0)) {
      float t0, t1;
      asm volatile("s_nop 7\n\ts_nop 7\n\t" W4_RESCALE_TEXT "s_nop 1" : "=&v"(t0), "=&v"(t1) : "v"(c.fct[0]), "v"(c.fct[1]) : W4_ACLOB);
      c.lsum[0] = fmul_s(c.lsum[0], c.fct[0]); c.lsum[1] = fmul_s(c.lsum[1], c.fct[1]); c.pend = false;
    }
  }
  asm volatile("s_waitcnt lgkmcnt(0)" ::: "memory");
  GAP();
}
template <int X, bool PEEL, bool ZERO1, bool DMA, bool KR> DI void phaseB(W4C& c, const int t) { phaseB_<X, PEEL, ZERO1, DMA, KR>(c, t, make_iseq<32>{}); }

template <int... Is> DI void tr_reads(W4C& c, unsigned vslot, iseq<Is...>) { (tr_read<Is>(c, vslot), ...); }
template <int X, int... Cs> DI void packs(W4C& c, iseq<Cs...>) { (pack1<X, ((Cs >> 3) & 1), 2 * (Cs >> 4) + ((Cs >> 2) & 1), (Cs & 3)>(c), ...); }
template <int X, int J> DI void rowsum1_padded(W4C& c) { rowsum1<X, J>(c); asm volatile("s_nop 1"); }
template <int X, int... Js> DI void rowsums(W4C& c, iseq<Js...>) { (rowsum1_padded<X, Js>(c), ...); }
template <int M> DI void pv_drain1(W4C& c) { pv_mfma<M, false>(c); GAP(); }
template <int... Ms> DI void pv_drain(W4C& c, iseq<Ms...>) { (pv_drain1<Ms>(c), ...); }

struct QC {
  unsigned vxb[4], vwb, vxsrc[2], vwsrc, m0x, m0w, sofx, sofw;
  __amdgpu_buffer_rsrc_t srdX, srdW;
  bf16x4 alo[2][4], aup[2][4];
  bf16x8 xb[2][2];
};
#define DMA_ASM(M0V, VOFFV, SRD, SOFF) asm volatile("s_mov_b32 m0, %0\n\ts_nop 0\n\tbuffer_load_dwordx4 %1, %2, %3 offen lds" :: "s"(M0V), "v"(VOFFV), "s"(SRD), "s"(SOFF) : "m0", "memory")
template <int CI, int P> DI void q_dma(QC& q) {
  if constexpr (P < 8) DMA_ASM(q.m0x + QBUF(CI) + 1024u * P, q.vxsrc[P & 1], q.srdX, q.sofx + 8192u * P + 128u * CI);
  else { constexpr int pp = P - 8; DMA_ASM(q.m0w + QBUF(CI) + 1024u * pp, q.vwsrc, q.srdW, q.sofw + 524288u * CI + 65536u * (pp >> 1) + 128u * (pp & 1)); }
}
template <int T, int P> DI void kv_dma(W4C& c) {
  if constexpr (P < 4) DMA_ASM(c.m0k + (T & 3) * SLOTB + 1024u * P, c.vksrc[P & 1], c.srdK, c.sofk + 16384u * T + 1024u * P);
  else { constexpr int pp = P - 4; DMA_ASM(c.m0v + (T & 3) * SLOTB + 1024u * pp, c.vvsrc, c.srdV, c.sofv + 16384u * T + 2048u * (pp >> 1) + 128u * (pp & 1)); }
}
template <int CI, int KS, int R> DI void q_read(QC& q) {
  constexpr int PAR = KS & 1;
  if constexpr (R < 8) {
    constexpr int db = R >> 1;
    if constexpr ((R & 1) == 0) asm volatile("ds_read_b64_tr_b16 %0, %1 offset:%2" : "=v"(q.alo[PAR][db]) : "v"(q.vwb + QBUF(CI)), "i"(4096 * KS + 512 * db) : "memory");
    else                        asm volatile("ds_read_b64_tr_b16 %0, %1 offset:%2" : "=v"(q.aup[PAR][db]) : "v"(q.vwb + QBUF(CI)), "i"(4096 * KS + 512 * db + 256) : "memory");
  } else {
    constexpr int qb = R - 8;
    asm volatile("ds_read_b128 %0, %1 offset:%2" : "=v"(q.xb[PAR][qb]) : "v"(q.vxb[KS] + QBUF(CI)), "i"(4096 * qb) : "memory");
  }
}
template <int CI, int KS, int M> DI void q_mfma(QC& q) {
  constexpr int PAR = KS & 1, db = M >> 1, qb = M & 1, ao = AO_(db, qb);
  const bf16x8 a = __builtin_shufflevector(q.alo[PAR][db], q.aup[PAR][db], 0, 1, 2, 3, 4, 5, 6, 7);
  if constexpr (CI == 0 && KS == 0) asm volatile("v_mfma_f32_32x32x16_bf16 a[%c2:%c3], %0, %1, 0" :: "v"(a), "v"(q.xb[PAR][qb]), "i"(ao), "i"(ao + 15) : W4_ACLOB);
  else                              asm volatile("v_mfma_f32_32x32x16_bf16 a[%c2:%c3], %0, %1, a[%c2:%c3]" :: "v"(a), "v"(q.xb[PAR][qb]), "i"(ao), "i"(ao + 15) : W4_ACLOB);
}
constexpr int KVN(int ci) { return ci < 4 ? 3 : (ci < 6 ? 2 : 0); }
constexpr int KV0(int ci) { return ci < 4 ? 3 * ci : (ci < 6 ? 12 + 2 * (ci - 4) : 16); }
template <int CI, int KS, int M> DI void q_gap(QC& q, W4C& c) {
  q_mfma<CI, KS, M>(q);
  constexpr int NCI = KS == 3 ? CI + 1 : CI, NKS = (KS + 1) & 3;
  if constexpr (NCI < 8 && M < 5) { q_read<NCI, NKS, 2 * M>(q); q_read<NCI, NKS, 2 * M + 1>(q); }
  if constexpr (KS == 3) {
    if constexpr (CI + 2 < 8) {
      if constexpr (M < 4) { q_dma<CI + 2, 2 * M>(q); q_dma<CI + 2, 2 * M + 1>(q); } else q_dma<CI + 2, M + 4>(q);
    } else kv_dma<CI - 4, M>(c);
    if constexpr (CI == 7) { k_read<2 * M>(c, 0u); k_read<2 * M + 1>(c, 0u); }
  } else if constexpr (KS < KVN(CI) && M == 6) {
    constexpr int idx = KV0(CI) + KS; kv_dma<(idx >> 3), (idx & 7)>(c);
  }
  GAP();
}
template <int CI, int KS, int... Ms> DI void q_step_(QC& q, W4C& c, iseq<Ms...>) {
  constexpr int PAR = KS & 1;
  if constexpr (KS == 3) {
    constexpr int VM = CI == 7 ? 8 : KVN(CI);
    asm volatile("s_waitcnt vmcnt(%10) lgkmcnt(0)\n\ts_barrier" : "+v"(q.alo[PAR][0]), "+v"(q.alo[PAR][1]), "+v"(q.alo[PAR][2]), "+v"(q.alo[PAR][3]),
                 "+v"(q.aup[PAR][0]), "+v"(q.aup[PAR][1]), "+v"(q.aup[PAR][2]), "+v"(q.aup[PAR][3]), "+v"(q.xb[PAR][0]), "+v"(q.xb[PAR][1]) : "i"(VM) : "memory");
  } else {
    asm volatile("s_waitcnt lgkmcnt(0)" : "+v"(q.alo[PAR][0]), "+v"(q.alo[PAR][1]), "+v"(q.alo[PAR][2]), "+v"(q.alo[PAR][3]),
                 "+v"(q.aup[PAR][0]), "+v"(q.aup[PAR][1]), "+v"(q.aup[PAR][2]), "+v"(q.aup[PAR][3]), "+v"(q.xb[PAR][0]), "+v"(q.xb[PAR][1]) :: "memory");
  }
  GAP();
  (q_gap<CI, KS, Ms>(q, c), ...);
}
template <int CI> DI void q_chunk(QC& q, W4C& c) {
  q_step_<CI, 0>(q, c, make_iseq<8>{}); q_step_<CI, 1>(q, c, make_iseq<8>{}); q_step_<CI, 2>(q, c, make_iseq<8>{}); q_step_<CI, 3>(q, c, make_iseq<8>{});
}
template <int... Cs> DI void q_chunks(QC& q, W4C& c, iseq<Cs...>) { (q_chunk<Cs>(q, c), ...); }
template <int CI, int... Ps> DI void q_dmas(QC& q, iseq<Ps...>) { (q_dma<CI, Ps>(q), ...); }
template <int... Rs> DI void q_reads0(QC& q, iseq<Rs...>) { (q_read<0, 0, Rs>(q), ...); }

template <int BASE> DI void acc_read8(float (&o)[8]) {
  asm volatile("v_accvgpr_read_b32 %0, a[%c8]\n\tv_accvgpr_read_b32 %1, a[%c9]\n\tv_accvgpr_read_b32 %2, a[%c10]\n\tv_accvgpr_read_b32 %3, a[%c11]\n\t"
               "v_accvgpr_read_b32 %4, a[%c12]\n\tv_accvgpr_read_b32 %5, a[%c13]\n\tv_accvgpr_read_b32 %6, a[%c14]\n\tv_accvgpr_read_b32 %7, a[%c15]"
               : "=v"(o[0]), "=v"(o[1]), "=v"(o[2]), "=v"(o[3]), "=v"(o[4]), "=v"(o[5]), "=v"(o[6]), "=v"(o[7])
               : "i"(BASE), "i"(BASE + 1), "i"(BASE + 2), "i"(BASE + 3), "i"(BASE + 4), "i"(BASE + 5), "i"(BASE + 6), "i"(BASE + 7));
}
template <int AQ> DI void acc_write4(const u32x4& w) {
  asm volatile("v_accvgpr_write_b32 a[%c4], %0\n\tv_accvgpr_write_b32 a[%c5], %1\n\tv_accvgpr_write_b32 a[%c6], %2\n\tv_accvgpr_write_b32 a[%c7], %3"
               :: "v"(w[0]), "v"(w[1]), "v"(w[2]), "v"(w[3]), "i"(AQ), "i"(AQ + 1), "i"(AQ + 2), "i"(AQ + 3) : W4_ACLOB_HI);
}
template <int I> DI void q_convert(float C) {
  constexpr int qb = I >> 3, d0 = I & 7, db = d0 >> 1, s = d0 & 1;
  float o[8]; acc_read8<AO_(db, qb) + 8 * s>(o);
  u32x4 w;
  #pragma unroll
  for (int e = 0; e < 4; ++e) w[e] = cvtpk(fmul_s(o[2 * e], C), fmul_s(o[2 * e + 1], C));
  acc_write4<AQ_(qb, d0)>(w);
}
template <int... Is> DI void q_converts(float C, iseq<Is...>) { (q_convert<Is>(C), ...); }
template <int I> DI void o_store(const float (&il)[2], char* obase, unsigned voff) {
  constexpr int qb = I >> 3, kk = I & 7, db = kk >> 1, s = kk & 1;
  float o[8]; acc_read8<AO_(db, qb) + 8 * s>(o);
  u32x4 w;
  #pragma unroll
  for (int e = 0; e < 4; ++e) w[e] = cvtpk(fmul_s(o[2 * e], il[qb]), fmul_s(o[2 * e + 1], il[qb]));
  *(u32x4*)(obase + (size_t)qb * (256 * 1024) + kk * 1024 + voff) = w;
}
template <int... Is> DI void o_stores(const float (&il)[2], char* obase, unsigned voff, iseq<Is...>) { (o_store<Is>(il, obase, voff), ...); }

constexpr size_t WS_OF = 0, WS_WOF = (size_t)16 << 20;

extern "C" __global__ __launch_bounds__(256, 1)
void attn_fused(const unsigned short* __restrict__ X, const unsigned short* __restrict__ Kc, const unsigned short* __restrict__ Vc,
                const unsigned short* __restrict__ Wq, const unsigned short* __restrict__ Wo, char* __restrict__ ws) {
  extern __shared__ __attribute__((aligned(16))) char lds[];
  const unsigned tid = threadIdx.x, lane = tid & 63u, wid = tid >> 6, l32 = lane & 31u, hi = lane >> 5;
  const unsigned g = blockIdx.x, hd = 4u * (g & 7u) + ((g >> 3) & 3u), b = g >> 5;
  const float C = 0.12751743082f;
  W4C c; QC q;
  c.ldsb = (unsigned)(size_t)lds;
  #pragma unroll
  for (int j = 0; j < 4; ++j) c.vkb[j] = c.ldsb + 256u * l32 + 16u * ((2u * j + hi) ^ (l32 & 7u));
  c.vvb = c.ldsb + VOFF + (((lane & 3u) << 3) | (((lane >> 2) & 3u) << 6) | (((lane >> 4) & 1u) << 5) | (hi << 8));
  #pragma unroll
  for (int par = 0; par < 2; ++par) c.vksrc[par] = 256u * (lane >> 4) + 16u * ((lane & 15u) ^ (4u * par + (lane >> 4)));
  c.vvsrc = 256u * ((lane >> 2) & 7u) + 64u * hi + 16u * (lane & 3u);
  c.m0k = rfl(c.ldsb + 4096u * wid); c.m0v = rfl(c.ldsb + VOFF + 4096u * wid);
  c.sofk = rfl(4096u * wid); c.sofv = rfl(4096u * wid);
  const size_t kvoff = ((size_t)b * NH + hd) * (SK * DH * 2);
  c.srdK = __builtin_amdgcn_make_buffer_rsrc((void*)((const char*)Kc + kvoff), (short)0, SK * DH * 2, SRD_WORD3);
  c.srdV = __builtin_amdgcn_make_buffer_rsrc((void*)((const char*)Vc + kvoff), (short)0, SK * DH * 2, SRD_WORD3);
  #pragma unroll
  for (int ks = 0; ks < 4; ++ks) q.vxb[ks] = c.ldsb + 8192u * wid + 128u * l32 + 16u * ((2u * ks + hi) ^ ((l32 >> 1) & 7u));
  { const unsigned p = lane & 3u, sp = ((p & 1u) << 1) | (p >> 1);
    q.vwb = c.ldsb + QWOFF + ((hi << 11) | (((lane >> 2) & 3u) << 6) | (((lane >> 4) & 1u) << 5) | (sp << 3)); }
  #pragma unroll
  for (int par = 0; par < 2; ++par) q.vxsrc[par] = 1024u * (lane >> 3) + 16u * ((lane & 7u) ^ (4u * par + (lane >> 4)));
  q.vwsrc = 8192u * ((lane >> 2) & 7u) + 64u * hi + 16u * (lane & 3u);
  q.m0x = rfl(c.ldsb + 8192u * wid); q.m0w = rfl(c.ldsb + QWOFF + 4096u * wid);
  q.sofx = rfl(65536u * wid); q.sofw = rfl(131072u * wid);
  q.srdX = __builtin_amdgcn_make_buffer_rsrc((void*)((const char*)X + (size_t)b * (SQ * DM * 2)), (short)0, SQ * DM * 2, SRD_WORD3);
  q.srdW = __builtin_amdgcn_make_buffer_rsrc((void*)((const char*)Wq + hd * 256u), (short)0, DM * NH * DH * 2 - (int)hd * 256, SRD_WORD3);
  asm volatile("s_nop 4" ::: "memory");
  q_dmas<0>(q, make_iseq<12>{});
  q_dmas<1>(q, make_iseq<12>{});
  {
    #pragma unroll
    for (int i = 0; i < 4; ++i) {
      const unsigned nb = 4u * wid + i;
      unsigned short v[8];
      #pragma unroll
      for (int j = 0; j < 8; ++j) v[j] = Wo[(size_t)(16u * g + 8u * (j >> 2) + 4u * hi + (j & 3)) * DM + 32u * nb + l32];
      u32x4 w;
      #pragma unroll
      for (int e = 0; e < 4; ++e) w[e] = (unsigned)v[2 * e] | ((unsigned)v[2 * e + 1] << 16);
      *(u32x4*)(ws + WS_WOF + ((size_t)nb * 256 + g) * 1024 + lane * 16u) = w;
    }
  }
  asm volatile("s_waitcnt vmcnt(0)\n\ts_barrier" ::: "memory");
  q_reads0(q, make_iseq<10>{});
  q_chunks(q, c, make_iseq<8>{});
  asm volatile("s_nop 7\n\ts_nop 7" ::: "memory");
  q_converts(C, make_iseq<16>{});
  asm volatile("s_waitcnt lgkmcnt(0)\n\ts_nop 1" ::: "memory");

  c.mhat[0] = c.mhat[1] = 0.f; c.lsum[0] = c.lsum[1] = 0.f; c.fct[0] = c.fct[1] = 1.f; c.pend = false;
  phaseA<0, true, false, 16>(c, 0);  phaseB<0, true,  false, false, true>(c, 0);
  phaseA<1, false, true, 12>(c, 1);  phaseB<1, false, true,  true,  true>(c, 1);
  for (int t = 2; t < 12; t += 2) {
    phaseA<0, false, true, 12>(c, t);     phaseB<0, false, false, true, true>(c, t);
    phaseA<1, false, true, 12>(c, t + 1); phaseB<1, false, false, true, true>(c, t + 1);
  }
  phaseA<0, false, true, 12>(c, 12);  phaseB<0, false, false, true,  true>(c, 12);
  phaseA<1, false, false, 12>(c, 13); phaseB<1, false, false, false, true>(c, 13);
  phaseA<0, false, false, 4>(c, 14);  phaseB<0, false, false, false, true>(c, 14);
  phaseA<1, false, false, 0>(c, 15);  phaseB<1, false, false, false, false>(c, 15);
  {
    const unsigned vslot = (unsigned)((NT - 1) & 3) * SLOTB;
    tr_reads(c, vslot, make_iseq<32>{});
    #pragma unroll
    for (int e = 0; e < 32; ++e) c.S[1][1][e >> 4][e & 15] = __builtin_amdgcn_exp2f(c.S[1][1][e >> 4][e & 15]);
    packs<1>(c, make_iseq<32>{});
    rowsums<1>(c, iseq<8, 9, 10, 11, 12, 13, 14, 15, 24, 25, 26, 27, 28, 29, 30, 31>{});
    #pragma unroll
    for (int qb = 0; qb < 2; ++qb) { const float rs = fadd_s(c.rs0[qb], c.rs1[qb]);
      auto pr = __builtin_amdgcn_permlane32_swap(__float_as_uint(rs), __float_as_uint(rs), false, false);
      c.lsum[qb] = fadd_s(c.lsum[qb], fadd_s(__uint_as_float(pr[0]), __uint_as_float(pr[1]))); }
    asm volatile("s_waitcnt lgkmcnt(0)" : W4_VLO :: "memory"); asm volatile("" : W4_VUP :: "memory");
    GAP();
    #pragma unroll
    for (int ks = 0; ks < 4; ++ks)
      #pragma unroll
      for (int db = 0; db < 4; ++db) c.Vf[db][ks] = __builtin_shufflevector(c.vlo[db][ks], c.vup[db][ks], 0, 1, 2, 3, 4, 5, 6, 7);
    asm volatile("s_nop 1" ::: "memory");
    pv_drain(c, make_iseq<32>{});
    asm volatile("s_nop 7\n\ts_nop 7" ::: "memory");
  }
  {
    float il[2];
    il[0] = __builtin_amdgcn_rcpf(c.lsum[0]); il[1] = __builtin_amdgcn_rcpf(c.lsum[1]);
    asm volatile("s_nop 1" : "+v"(il[0]), "+v"(il[1]));
    char* obase = ws + WS_OF + ((size_t)(b * 8u + 2u * wid) * 256 + hd * 8u) * 1024;
    o_stores(il, obase, lane * 16u, make_iseq<16>{});
  }
}

extern "C" __global__ __launch_bounds__(512, 2)
void oproj(const char* __restrict__ ws, unsigned short* __restrict__ out) {
  __shared__ __attribute__((aligned(16))) float red[8 * 64 * 68];
  const unsigned tid = threadIdx.x, lane = tid & 63u, wid = tid >> 6, l32 = lane & 31u, hi = lane >> 5;
  const unsigned g = blockIdx.x, xc = g & 7u, idx = g >> 3;
  const unsigned qt = 4u * xc + (idx & 3u), nt = idx >> 2;
  const char* ofp = ws + WS_OF + ((size_t)(2u * qt) * 256 + 32u * wid) * 1024 + lane * 16u;
  const char* wfp = ws + WS_WOF + ((size_t)(2u * nt) * 256 + 32u * wid) * 1024 + lane * 16u;
  f32x16 acc[2][2] = {};
  bf16x8 fo[4][2], fw[4][2];
  #pragma unroll
  for (int i = 0; i < 4; ++i) {
    fo[i][0] = *(const bf16x8*)(ofp + i * 1024); fo[i][1] = *(const bf16x8*)(ofp + 262144 + i * 1024);
    fw[i][0] = *(const bf16x8*)(wfp + i * 1024); fw[i][1] = *(const bf16x8*)(wfp + 262144 + i * 1024);
  }
  #pragma unroll 1
  for (int k0 = 0; k0 < 32; k0 += 4) {
    #pragma unroll
    for (int i = 0; i < 4; ++i) {
      const bf16x8 o0 = fo[i][0], o1 = fo[i][1], w0 = fw[i][0], w1 = fw[i][1];
      if (k0 + 4 < 32) {
        const int kn = k0 + 4 + i;
        fo[i][0] = *(const bf16x8*)(ofp + kn * 1024); fo[i][1] = *(const bf16x8*)(ofp + 262144 + kn * 1024);
        fw[i][0] = *(const bf16x8*)(wfp + kn * 1024); fw[i][1] = *(const bf16x8*)(wfp + 262144 + kn * 1024);
      }
      acc[0][0] = __builtin_amdgcn_mfma_f32_32x32x16_bf16(w0, o0, acc[0][0], 0, 0, 0);
      acc[0][1] = __builtin_amdgcn_mfma_f32_32x32x16_bf16(w0, o1, acc[0][1], 0, 0, 0);
      acc[1][0] = __builtin_amdgcn_mfma_f32_32x32x16_bf16(w1, o0, acc[1][0], 0, 0, 0);
      acc[1][1] = __builtin_amdgcn_mfma_f32_32x32x16_bf16(w1, o1, acc[1][1], 0, 0, 0);
    }
  }
  float* rw = red + wid * (64 * 68);
  #pragma unroll
  for (int nb = 0; nb < 2; ++nb)
    #pragma unroll
    for (int qb = 0; qb < 2; ++qb)
      #pragma unroll
      for (int gq = 0; gq < 4; ++gq) {
        f32x4 v = {acc[nb][qb][4 * gq], acc[nb][qb][4 * gq + 1], acc[nb][qb][4 * gq + 2], acc[nb][qb][4 * gq + 3]};
        *(f32x4*)(rw + (32 * qb + l32) * 68 + 32 * nb + 8 * gq + 4 * hi) = v;
      }
  __syncthreads();
  {
    const unsigned qr = tid >> 3, n0 = (tid & 7u) * 8u;
    f32x4 s0 = {}, s1 = {};
    #pragma unroll
    for (int w = 0; w < 8; ++w) {
      s0 += *(const f32x4*)(red + w * (64 * 68) + qr * 68 + n0);
      s1 += *(const f32x4*)(red + w * (64 * 68) + qr * 68 + n0 + 4);
    }
    u32x4 w; w[0] = cvtpk(s0[0], s0[1]); w[1] = cvtpk(s0[2], s0[3]); w[2] = cvtpk(s1[0], s1[1]); w[3] = cvtpk(s1[2], s1[3]);
    *(u32x4*)(out + (size_t)(64u * qt + qr) * DM + 64u * nt + n0) = w;
  }
}

extern "C" void kernel_launch(void* const* d_in, const int* in_sizes, int n_in, void* d_out, int out_size, void* d_ws, size_t ws_size, hipStream_t stream) {
  (void)in_sizes; (void)n_in; (void)out_size; (void)ws_size;
  (void)hipFuncSetAttribute((const void*)attn_fused, hipFuncAttributeMaxDynamicSharedMemorySize, (int)SHM_ATTN);
  hipLaunchKernelGGL(attn_fused, dim3(256), dim3(256), SHM_ATTN, stream, (const unsigned short*)d_in[0], (const unsigned short*)d_in[1], (const unsigned short*)d_in[2],
                     (const unsigned short*)d_in[3], (const unsigned short*)d_in[4], (char*)d_ws);
  hipLaunchKernelGGL(oproj, dim3(256), dim3(512), 0, stream, (const char*)d_ws, (unsigned short*)d_out);
}
```

```cpp
#include <hip/hip_runtime.h>
#include <utility>

constexpr int NB = 8, SQ = 256, DM = 512, NH = 32, SK = 1024, DH = 128, NT = SK / 64;
constexpr float THR = 8.0f;
constexpr size_t SHM_ATTN = 163840;
typedef float    f32x16 __attribute__((ext_vector_type(16)));
typedef float    f32x2  __attribute__((ext_vector_type(2)));
typedef float    f32x4  __attribute__((ext_vector_type(4)));
typedef __bf16   bf16x8 __attribute__((ext_vector_type(8)));
typedef __bf16   bf16x4 __attribute__((ext_vector_type(4)));
typedef __bf16   bf16x2 __attribute__((ext_vector_type(2)));
typedef unsigned u32x4  __attribute__((ext_vector_type(4)));
typedef unsigned u32x2  __attribute__((ext_vector_type(2)));
template <int... Is> using iseq = std::integer_sequence<int, Is...>;
template <int N> using make_iseq = std::make_integer_sequence<int, N>;

#define SLOTB 0x8000u
#define KOFF  0x4000u
#define QBUF(i) (49152u * ((i) % 3))
__device__ __forceinline__ constexpr unsigned tslot(unsigned t) { return ((t + 4u) % 5u) * SLOTB; }
#define QWOFF 32768u
#define SRD_WORD3 0x00020000
#define DI __device__ __forceinline__
#define GAP() __builtin_amdgcn_sched_barrier(0)
#define W4_ACLOB "a0","a1","a2","a3","a4","a5","a6","a7","a8","a9","a10","a11","a12","a13","a14","a15","a16","a17","a18","a19","a20","a21","a22","a23","a24","a25","a26","a27","a28","a29","a30","a31","a32","a33","a34","a35","a36","a37","a38","a39","a40","a41","a42","a43","a44","a45","a46","a47","a48","a49","a50","a51","a52","a53","a54","a55","a56","a57","a58","a59","a60","a61","a62","a63","a64","a65","a66","a67","a68","a69","a70","a71","a72","a73","a74","a75","a76","a77","a78","a79","a80","a81","a82","a83","a84","a85","a86","a87","a88","a89","a90","a91","a92","a93","a94","a95","a96","a97","a98","a99","a100","a101","a102","a103","a104","a105","a106","a107","a108","a109","a110","a111","a112","a113","a114","a115","a116","a117","a118","a119","a120","a121","a122","a123","a124","a125","a126","a127"
#define W4_ACLOB_HI "a128","a129","a130","a131","a132","a133","a134","a135","a136","a137","a138","a139","a140","a141","a142","a143","a144","a145","a146","a147","a148","a149","a150","a151","a152","a153","a154","a155","a156","a157","a158","a159","a160","a161","a162","a163","a164","a165","a166","a167","a168","a169","a170","a171","a172","a173","a174","a175","a176","a177","a178","a179","a180","a181","a182","a183","a184","a185","a186","a187","a188","a189","a190","a191","a192","a193","a194","a195","a196","a197","a198","a199","a200","a201","a202","a203","a204","a205","a206","a207","a208","a209","a210","a211","a212","a213","a214","a215","a216","a217","a218","a219","a220","a221","a222","a223","a224","a225","a226","a227","a228","a229","a230","a231","a232","a233","a234","a235","a236","a237","a238","a239","a240","a241","a242","a243","a244","a245","a246","a247","a248","a249","a250","a251","a252","a253","a254","a255"
#define W4_RESCALE_TEXT "v_accvgpr_read_b32 %0, a0\n\tv_mul_f32 %0, %2, %0\n\tv_accvgpr_write_b32 a0, %0\n\t" "v_accvgpr_read_b32 %1, a1\n\tv_mul_f32 %1, %2, %1\n\tv_accvgpr_write_b32 a1, %1\n\t" "v_accvgpr_read_b32 %0, a2\n\tv_mul_f32 %0, %2, %0\n\tv_accvgpr_write_b32 a2, %0\n\t" "v_accvgpr_read_b32 %1, a3\n\tv_mul_f32 %1, %2, %1\n\tv_accvgpr_write_b32 a3, %1\n\t" "v_accvgpr_read_b32 %0, a4\n\tv_mul_f32 %0, %2, %0\n\tv_accvgpr_write_b32 a4, %0\n\t" "v_accvgpr_read_b32 %1, a5\n\tv_mul_f32 %1, %2, %1\n\tv_accvgpr_write_b32 a5, %1\n\t" "v_accvgpr_read_b32 %0, a6\n\tv_mul_f32 %0, %2, %0\n\tv_accvgpr_write_b32 a6, %0\n\t" "v_accvgpr_read_b32 %1, a7\n\tv_mul_f32 %1, %2, %1\n\tv_accvgpr_write_b32 a7, %1\n\t" "v_accvgpr_read_b32 %0, a8\n\tv_mul_f32 %0, %2, %0\n\tv_accvgpr_write_b32 a8, %0\n\t" "v_accvgpr_read_b32 %1, a9\n\tv_mul_f32 %1, %2, %1\n\tv_accvgpr_write_b32 a9, %1\n\t" "v_accvgpr_read_b32 %0, a10\n\tv_mul_f32 %0, %2, %0\n\tv_accvgpr_write_b32 a10, %0\n\t" "v_accvgpr_read_b32 %1, a11\n\tv_mul_f32 %1, %2, %1\n\tv_accvgpr_write_b32 a11, %1\n\t" "v_accvgpr_read_b32 %0, a12\n\tv_mul_f32 %0, %2, %0\n\tv_accvgpr_write_b32 a12, %0\n\t" "v_accvgpr_read_b32 %1, a13\n\tv_mul_f32 %1, %2, %1\n\tv_accvgpr_write_b32 a13, %1\n\t" "v_accvgpr_read_b32 %0, a14\n\tv_mul_f32 %0, %2, %0\n\tv_accvgpr_write_b32 a14, %0\n\t" "v_accvgpr_read_b32 %1, a15\n\tv_mul_f32 %1, %2, %1\n\tv_accvgpr_write_b32 a15, %1\n\t" "v_accvgpr_read_b32 %0, a16\n\tv_mul_f32 %0, %3, %0\n\tv_accvgpr_write_b32 a16, %0\n\t" "v_accvgpr_read_b32 %1, a17\n\tv_mul_f32 %1, %3, %1\n\tv_accvgpr_write_b32 a17, %1\n\t" "v_accvgpr_read_b32 %0, a18\n\tv_mul_f32 %0, %3, %0\n\tv_accvgpr_write_b32 a18, %0\n\t" "v_accvgpr_read_b32 %1, a19\n\tv_mul_f32 %1, %3, %1\n\tv_accvgpr_write_b32 a19, %1\n\t" "v_accvgpr_read_b32 %0, a20\n\tv_mul_f32 %0, %3, %0\n\tv_accvgpr_write_b32 a20, %0\n\t" "v_accvgpr_read_b32 %1, a21\n\tv_mul_f32 %1, %3, %1\n\tv_accvgpr_write_b32 a21, %1\n\t" "v_accvgpr_read_b32 %0, a22\n\tv_mul_f32 %0, %3, %0\n\tv_accvgpr_write_b32 a22, %0\n\t" "v_accvgpr_read_b32 %1, a23\n\tv_mul_f32 %1, %3, %1\n\tv_accvgpr_write_b32 a23, %1\n\t" "v_accvgpr_read_b32 %0, a24\n\tv_mul_f32 %0, %3, %0\n\tv_accvgpr_write_b32 a24, %0\n\t" "v_accvgpr_read_b32 %1, a25\n\tv_mul_f32 %1, %3, %1\n\tv_accvgpr_write_b32 a25, %1\n\t" "v_accvgpr_read_b32 %0, a26\n\tv_mul_f32 %0, %3, %0\n\tv_accvgpr_write_b32 a26, %0\n\t" "v_accvgpr_read_b32 %1, a27\n\tv_mul_f32 %1, %3, %1\n\tv_accvgpr_write_b32 a27, %1\n\t" "v_accvgpr_read_b32 %0, a28\n\tv_mul_f32 %0, %3, %0\n\tv_accvgpr_write_b32 a28, %0\n\t" "v_accvgpr_read_b32 %1, a29\n\tv_mul_f32 %1, %3, %1\n\tv_accvgpr_write_b32 a29, %1\n\t" "v_accvgpr_read_b32 %0, a30\n\tv_mul_f32 %0, %3, %0\n\tv_accvgpr_write_b32 a30, %0\n\t" "v_accvgpr_read_b32 %1, a31\n\tv_mul_f32 %1, %3, %1\n\tv_accvgpr_write_b32 a31, %1\n\t" "v_accvgpr_read_b32 %0, a32\n\tv_mul_f32 %0, %2, %0\n\tv_accvgpr_write_b32 a32, %0\n\t" "v_accvgpr_read_b32 %1, a33\n\tv_mul_f32 %1, %2, %1\n\tv_accvgpr_write_b32 a33, %1\n\t" "v_accvgpr_read_b32 %0, a34\n\tv_mul_f32 %0, %2, %0\n\tv_accvgpr_write_b32 a34, %0\n\t" "v_accvgpr_read_b32 %1, a35\n\tv_mul_f32 %1, %2, %1\n\tv_accvgpr_write_b32 a35, %1\n\t" "v_accvgpr_read_b32 %0, a36\n\tv_mul_f32 %0, %2, %0\n\tv_accvgpr_write_b32 a36, %0\n\t" "v_accvgpr_read_b32 %1, a37\n\tv_mul_f32 %1, %2, %1\n\tv_accvgpr_write_b32 a37, %1\n\t" "v_accvgpr_read_b32 %0, a38\n\tv_mul_f32 %0, %2, %0\n\tv_accvgpr_write_b32 a38, %0\n\t" "v_accvgpr_read_b32 %1, a39\n\tv_mul_f32 %1, %2, %1\n\tv_accvgpr_write_b32 a39, %1\n\t" "v_accvgpr_read_b32 %0, a40\n\tv_mul_f32 %0, %2, %0\n\tv_accvgpr_write_b32 a40, %0\n\t" "v_accvgpr_read_b32 %1, a41\n\tv_mul_f32 %1, %2, %1\n\tv_accvgpr_write_b32 a41, %1\n\t" "v_accvgpr_read_b32 %0, a42\n\tv_mul_f32 %0, %2, %0\n\tv_accvgpr_write_b32 a42, %0\n\t" "v_accvgpr_read_b32 %1, a43\n\tv_mul_f32 %1, %2, %1\n\tv_accvgpr_write_b32 a43, %1\n\t" "v_accvgpr_read_b32 %0, a44\n\tv_mul_f32 %0, %2, %0\n\tv_accvgpr_write_b32 a44, %0\n\t" "v_accvgpr_read_b32 %1, a45\n\tv_mul_f32 %1, %2, %1\n\tv_accvgpr_write_b32 a45, %1\n\t" "v_accvgpr_read_b32 %0, a46\n\tv_mul_f32 %0, %2, %0\n\tv_accvgpr_write_b32 a46, %0\n\t" "v_accvgpr_read_b32 %1, a47\n\tv_mul_f32 %1, %2, %1\n\tv_accvgpr_write_b32 a47, %1\n\t" "v_accvgpr_read_b32 %0, a48\n\tv_mul_f32 %0, %3, %0\n\tv_accvgpr_write_b32 a48, %0\n\t" "v_accvgpr_read_b32 %1, a49\n\tv_mul_f32 %1, %3, %1\n\tv_accvgpr_write_b32 a49, %1\n\t" "v_accvgpr_read_b32 %0, a50\n\tv_mul_f32 %0, %3, %0\n\tv_accvgpr_write_b32 a50, %0\n\t" "v_accvgpr_read_b32 %1, a51\n\tv_mul_f32 %1, %3, %1\n\tv_accvgpr_write_b32 a51, %1\n\t" "v_accvgpr_read_b32 %0, a52\n\tv_mul_f32 %0, %3, %0\n\tv_accvgpr_write_b32 a52, %0\n\t" "v_accvgpr_read_b32 %1, a53\n\tv_mul_f32 %1, %3, %1\n\tv_accvgpr_write_b32 a53, %1\n\t" "v_accvgpr_read_b32 %0, a54\n\tv_mul_f32 %0, %3, %0\n\tv_accvgpr_write_b32 a54, %0\n\t" "v_accvgpr_read_b32 %1, a55\n\tv_mul_f32 %1, %3, %1\n\tv_accvgpr_write_b32 a55, %1\n\t" "v_accvgpr_read_b32 %0, a56\n\tv_mul_f32 %0, %3, %0\n\tv_accvgpr_write_b32 a56, %0\n\t" "v_accvgpr_read_b32 %1, a57\n\tv_mul_f32 %1, %3, %1\n\tv_accvgpr_write_b32 a57, %1\n\t" "v_accvgpr_read_b32 %0, a58\n\tv_mul_f32 %0, %3, %0\n\tv_accvgpr_write_b32 a58, %0\n\t" "v_accvgpr_read_b32 %1, a59\n\tv_mul_f32 %1, %3, %1\n\tv_accvgpr_write_b32 a59, %1\n\t" "v_accvgpr_read_b32 %0, a60\n\tv_mul_f32 %0, %3, %0\n\tv_accvgpr_write_b32 a60, %0\n\t" "v_accvgpr_read_b32 %1, a61\n\tv_mul_f32 %1, %3, %1\n\tv_accvgpr_write_b32 a61, %1\n\t" "v_accvgpr_read_b32 %0, a62\n\tv_mul_f32 %0, %3, %0\n\tv_accvgpr_write_b32 a62, %0\n\t" "v_accvgpr_read_b32 %1, a63\n\tv_mul_f32 %1, %3, %1\n\tv_accvgpr_write_b32 a63, %1\n\t" "v_accvgpr_read_b32 %0, a64\n\tv_mul_f32 %0, %2, %0\n\tv_accvgpr_write_b32 a64, %0\n\t" "v_accvgpr_read_b32 %1, a65\n\tv_mul_f32 %1, %2, %1\n\tv_accvgpr_write_b32 a65, %1\n\t" "v_accvgpr_read_b32 %0, a66\n\tv_mul_f32 %0, %2, %0\n\tv_accvgpr_write_b32 a66, %0\n\t" "v_accvgpr_read_b32 %1, a67\n\tv_mul_f32 %1, %2, %1\n\tv_accvgpr_write_b32 a67, %1\n\t" "v_accvgpr_read_b32 %0, a68\n\tv_mul_f32 %0, %2, %0\n\tv_accvgpr_write_b32 a68, %0\n\t" "v_accvgpr_read_b32 %1, a69\n\tv_mul_f32 %1, %2, %1\n\tv_accvgpr_write_b32 a69, %1\n\t" "v_accvgpr_read_b32 %0, a70\n\tv_mul_f32 %0, %2, %0\n\tv_accvgpr_write_b32 a70, %0\n\t" "v_accvgpr_read_b32 %1, a71\n\tv_mul_f32 %1, %2, %1\n\tv_accvgpr_write_b32 a71, %1\n\t" "v_accvgpr_read_b32 %0, a72\n\tv_mul_f32 %0, %2, %0\n\tv_accvgpr_write_b32 a72, %0\n\t" "v_accvgpr_read_b32 %1, a73\n\tv_mul_f32 %1, %2, %1\n\tv_accvgpr_write_b32 a73, %1\n\t" "v_accvgpr_read_b32 %0, a74\n\tv_mul_f32 %0, %2, %0\n\tv_accvgpr_write_b32 a74, %0\n\t" "v_accvgpr_read_b32 %1, a75\n\tv_mul_f32 %1, %2, %1\n\tv_accvgpr_write_b32 a75, %1\n\t" "v_accvgpr_read_b32 %0, a76\n\tv_mul_f32 %0, %2, %0\n\tv_accvgpr_write_b32 a76, %0\n\t" "v_accvgpr_read_b32 %1, a77\n\tv_mul_f32 %1, %2, %1\n\tv_accvgpr_write_b32 a77, %1\n\t" "v_accvgpr_read_b32 %0, a78\n\tv_mul_f32 %0, %2, %0\n\tv_accvgpr_write_b32 a78, %0\n\t" "v_accvgpr_read_b32 %1, a79\n\tv_mul_f32 %1, %2, %1\n\tv_accvgpr_write_b32 a79, %1\n\t" "v_accvgpr_read_b32 %0, a80\n\tv_mul_f32 %0, %3, %0\n\tv_accvgpr_write_b32 a80, %0\n\t" "v_accvgpr_read_b32 %1, a81\n\tv_mul_f32 %1, %3, %1\n\tv_accvgpr_write_b32 a81, %1\n\t" "v_accvgpr_read_b32 %0, a82\n\tv_mul_f32 %0, %3, %0\n\tv_accvgpr_write_b32 a82, %0\n\t" "v_accvgpr_read_b32 %1, a83\n\tv_mul_f32 %1, %3, %1\n\tv_accvgpr_write_b32 a83, %1\n\t" "v_accvgpr_read_b32 %0, a84\n\tv_mul_f32 %0, %3, %0\n\tv_accvgpr_write_b32 a84, %0\n\t" "v_accvgpr_read_b32 %1, a85\n\tv_mul_f32 %1, %3, %1\n\tv_accvgpr_write_b32 a85, %1\n\t" "v_accvgpr_read_b32 %0, a86\n\tv_mul_f32 %0, %3, %0\n\tv_accvgpr_write_b32 a86, %0\n\t" "v_accvgpr_read_b32 %1, a87\n\tv_mul_f32 %1, %3, %1\n\tv_accvgpr_write_b32 a87, %1\n\t" "v_accvgpr_read_b32 %0, a88\n\tv_mul_f32 %0, %3, %0\n\tv_accvgpr_write_b32 a88, %0\n\t" "v_accvgpr_read_b32 %1, a89\n\tv_mul_f32 %1, %3, %1\n\tv_accvgpr_write_b32 a89, %1\n\t" "v_accvgpr_read_b32 %0, a90\n\tv_mul_f32 %0, %3, %0\n\tv_accvgpr_write_b32 a90, %0\n\t" "v_accvgpr_read_b32 %1, a91\n\tv_mul_f32 %1, %3, %1\n\tv_accvgpr_write_b32 a91, %1\n\t" "v_accvgpr_read_b32 %0, a92\n\tv_mul_f32 %0, %3, %0\n\tv_accvgpr_write_b32 a92, %0\n\t" "v_accvgpr_read_b32 %1, a93\n\tv_mul_f32 %1, %3, %1\n\tv_accvgpr_write_b32 a93, %1\n\t" "v_accvgpr_read_b32 %0, a94\n\tv_mul_f32 %0, %3, %0\n\tv_accvgpr_write_b32 a94, %0\n\t" "v_accvgpr_read_b32 %1, a95\n\tv_mul_f32 %1, %3, %1\n\tv_accvgpr_write_b32 a95, %1\n\t" "v_accvgpr_read_b32 %0, a96\n\tv_mul_f32 %0, %2, %0\n\tv_accvgpr_write_b32 a96, %0\n\t" "v_accvgpr_read_b32 %1, a97\n\tv_mul_f32 %1, %2, %1\n\tv_accvgpr_write_b32 a97, %1\n\t" "v_accvgpr_read_b32 %0, a98\n\tv_mul_f32 %0, %2, %0\n\tv_accvgpr_write_b32 a98, %0\n\t" "v_accvgpr_read_b32 %1, a99\n\tv_mul_f32 %1, %2, %1\n\tv_accvgpr_write_b32 a99, %1\n\t" "v_accvgpr_read_b32 %0, a100\n\tv_mul_f32 %0, %2, %0\n\tv_accvgpr_write_b32 a100, %0\n\t" "v_accvgpr_read_b32 %1, a101\n\tv_mul_f32 %1, %2, %1\n\tv_accvgpr_write_b32 a101, %1\n\t" "v_accvgpr_read_b32 %0, a102\n\tv_mul_f32 %0, %2, %0\n\tv_accvgpr_write_b32 a102, %0\n\t" "v_accvgpr_read_b32 %1, a103\n\tv_mul_f32 %1, %2, %1\n\tv_accvgpr_write_b32 a103, %1\n\t" "v_accvgpr_read_b32 %0, a104\n\tv_mul_f32 %0, %2, %0\n\tv_accvgpr_write_b32 a104, %0\n\t" "v_accvgpr_read_b32 %1, a105\n\tv_mul_f32 %1, %2, %1\n\tv_accvgpr_write_b32 a105, %1\n\t" "v_accvgpr_read_b32 %0, a106\n\tv_mul_f32 %0, %2, %0\n\tv_accvgpr_write_b32 a106, %0\n\t" "v_accvgpr_read_b32 %1, a107\n\tv_mul_f32 %1, %2, %1\n\tv_accvgpr_write_b32 a107, %1\n\t" "v_accvgpr_read_b32 %0, a108\n\tv_mul_f32 %0, %2, %0\n\tv_accvgpr_write_b32 a108, %0\n\t" "v_accvgpr_read_b32 %1, a109\n\tv_mul_f32 %1, %2, %1\n\tv_accvgpr_write_b32 a109, %1\n\t" "v_accvgpr_read_b32 %0, a110\n\tv_mul_f32 %0, %2, %0\n\tv_accvgpr_write_b32 a110, %0\n\t" "v_accvgpr_read_b32 %1, a111\n\tv_mul_f32 %1, %2, %1\n\tv_accvgpr_write_b32 a111, %1\n\t" "v_accvgpr_read_b32 %0, a112\n\tv_mul_f32 %0, %3, %0\n\tv_accvgpr_write_b32 a112, %0\n\t" "v_accvgpr_read_b32 %1, a113\n\tv_mul_f32 %1, %3, %1\n\tv_accvgpr_write_b32 a113, %1\n\t" "v_accvgpr_read_b32 %0, a114\n\tv_mul_f32 %0, %3, %0\n\tv_accvgpr_write_b32 a114, %0\n\t" "v_accvgpr_read_b32 %1, a115\n\tv_mul_f32 %1, %3, %1\n\tv_accvgpr_write_b32 a115, %1\n\t" "v_accvgpr_read_b32 %0, a116\n\tv_mul_f32 %0, %3, %0\n\tv_accvgpr_write_b32 a116, %0\n\t" "v_accvgpr_read_b32 %1, a117\n\tv_mul_f32 %1, %3, %1\n\tv_accvgpr_write_b32 a117, %1\n\t" "v_accvgpr_read_b32 %0, a118\n\tv_mul_f32 %0, %3, %0\n\tv_accvgpr_write_b32 a118, %0\n\t" "v_accvgpr_read_b32 %1, a119\n\tv_mul_f32 %1, %3, %1\n\tv_accvgpr_write_b32 a119, %1\n\t" "v_accvgpr_read_b32 %0, a120\n\tv_mul_f32 %0, %3, %0\n\tv_accvgpr_write_b32 a120, %0\n\t" "v_accvgpr_read_b32 %1, a121\n\tv_mul_f32 %1, %3, %1\n\tv_accvgpr_write_b32 a121, %1\n\t" "v_accvgpr_read_b32 %0, a122\n\tv_mul_f32 %0, %3, %0\n\tv_accvgpr_write_b32 a122, %0\n\t" "v_accvgpr_read_b32 %1, a123\n\tv_mul_f32 %1, %3, %1\n\tv_accvgpr_write_b32 a123, %1\n\t" "v_accvgpr_read_b32 %0, a124\n\tv_mul_f32 %0, %3, %0\n\tv_accvgpr_write_b32 a124, %0\n\t" "v_accvgpr_read_b32 %1, a125\n\tv_mul_f32 %1, %3, %1\n\tv_accvgpr_write_b32 a125, %1\n\t" "v_accvgpr_read_b32 %0, a126\n\tv_mul_f32 %0, %3, %0\n\tv_accvgpr_write_b32 a126, %0\n\t" "v_accvgpr_read_b32 %1, a127\n\tv_mul_f32 %1, %3, %1\n\tv_accvgpr_write_b32 a127, %1\n\t"

#define STAMP(i)

DI unsigned rfl(unsigned x) { return __builtin_amdgcn_readfirstlane(x); }
DI unsigned cvtpk(float lo, float hi) {
  f32x2 v = {lo, hi}; bf16x2 b = __builtin_convertvector(v, bf16x2); return __builtin_bit_cast(unsigned, b);
}
DI float max3f(float a, float b, float c) { float r; asm("v_max3_f32 %0, %1, %2, %3" : "=v"(r) : "v"(a), "v"(b), "v"(c)); return r; }
DI float max2f(float a, float b) { float r; asm("v_max_f32_e32 %0, %1, %2" : "=v"(r) : "v"(a), "v"(b)); return r; }
DI float fadd_s(float a, float b) { float r; asm("v_add_f32_e32 %0, %1, %2" : "=v"(r) : "v"(a), "v"(b)); return r; }
DI float fmul_s(float a, float b) { float r; asm("v_mul_f32_e32 %0, %1, %2" : "=v"(r) : "v"(a), "v"(b)); return r; }
DI float fsub_s(float a, float b) { float r; asm("v_sub_f32_e32 %0, %1, %2" : "=v"(r) : "v"(a), "v"(b)); return r; }
DI float w4_aone() { unsigned l; asm volatile("v_mbcnt_lo_u32_b32 %0, -1, 0\n\tv_mbcnt_hi_u32_b32 %0, -1, %0" : "=v"(l)); return l < 32u ? 1.f : 0.f; }
DI void negm_bcast(f32x16& negm, float aone, float neg_mhat) {
  asm volatile("s_nop 1\n\tv_mfma_f32_32x32x2_f32 %0, %1, %2, 0" : "+v"(negm) : "v"(aone), "v"(neg_mhat));
}

struct W4C {
  unsigned vkb[4], vvb, vksrc[2], vvsrc, m0k, m0v, sofk, sofv, ldsb;
  __amdgpu_buffer_rsrc_t srdK, srdV;
  f32x16 S[2][2][2];
  f32x16 NEGM[2];
  bf16x8 Vf[4][4];
  bf16x8 Pk[2][4];
  bf16x4 vlo[4][4], vup[4][4];
  float mhat[2], lsum[2], fct[2]; bool pend;
  float rs0[2], rs1[2], pmx0[2], pmx1[2], rmax[2];
};

constexpr int AO_(int db, int qb) { return 16 * (2 * db + qb); }
constexpr int AQ_(int qb, int d0) { return 128 + 32 * qb + 4 * d0; }
constexpr int AK_(int kb, int d0) { return 192 + 32 * kb + 4 * d0; }

template <int I> DI void tr_read(W4C& c, unsigned vslot) {
  constexpr int ks = I >> 3, db = (I >> 1) & 3;
  if constexpr ((I & 1) == 0) asm volatile("ds_read_b64_tr_b16 %0, %1 offset:%2" : "=v"(c.vlo[db][ks]) : "v"(c.vvb + vslot), "i"(4096 * ks + 512 * db) : "memory");
  else                        asm volatile("ds_read_b64_tr_b16 %0, %1 offset:%2" : "=v"(c.vup[db][ks]) : "v"(c.vvb + vslot), "i"(4096 * ks + 2048 + 512 * db) : "memory");
}
template <int I> DI void k_read(W4C& c, unsigned slot) {
  constexpr int kb = I >> 3, d0 = I & 7;
  asm volatile("ds_read_b128 a[%c1:%c2], %0 offset:%c3" :: "v"(c.vkb[d0 & 3] + slot), "i"(AK_(kb, d0)), "i"(AK_(kb, d0) + 3), "i"(8192 * kb + 128 * (d0 >> 2)) : "memory", W4_ACLOB_HI);
}
template <int X, bool FIRST, int M> DI void qk_mfma(W4C& c) {
  constexpr int d0 = M >> 2, kb = (M >> 1) & 1, qb = M & 1, ak = AK_(kb, d0), aq = AQ_(qb, d0);
  if constexpr (d0 == 0) {
    if constexpr (FIRST) asm volatile("v_mfma_f32_32x32x16_bf16 %0, a[%c1:%c2], a[%c3:%c4], 0" : "=&v"(c.S[X][kb][qb]) : "i"(ak), "i"(ak + 3), "i"(aq), "i"(aq + 3) : W4_ACLOB, W4_ACLOB_HI);
    else                 asm volatile("v_mfma_f32_32x32x16_bf16 %0, a[%c2:%c3], a[%c4:%c5], %1" : "=&v"(c.S[X][kb][qb]) : "v"(c.NEGM[qb]), "i"(ak), "i"(ak + 3), "i"(aq), "i"(aq + 3) : W4_ACLOB, W4_ACLOB_HI);
  } else asm volatile("v_mfma_f32_32x32x16_bf16 %0, a[%c1:%c2], a[%c3:%c4], %0" : "+v"(c.S[X][kb][qb]) : "i"(ak), "i"(ak + 3), "i"(aq), "i"(aq + 3) : W4_ACLOB, W4_ACLOB_HI);
}
template <int M, bool ZERO> DI void pv_mfma(W4C& c) {
  constexpr int ks = M >> 3, db = (M >> 1) & 3, qb = M & 1, ao = AO_(db, qb);
  if constexpr (ZERO && ks == 0) asm volatile("v_mfma_f32_32x32x16_bf16 a[%c2:%c3], %0, %1, 0" :: "v"(c.Vf[db][ks]), "v"(c.Pk[qb][ks]), "i"(ao), "i"(ao + 15) : W4_ACLOB);
  else                         asm volatile("v_mfma_f32_32x32x16_bf16 a[%c2:%c3], %0, %1, a[%c2:%c3]" :: "v"(c.Vf[db][ks]), "v"(c.Pk[qb][ks]), "i"(ao), "i"(ao + 15) : W4_ACLOB);
}
template <int X, int QB, int KS, int P> DI void pack1(W4C& c) {
  u32x4 w = __builtin_bit_cast(u32x4, c.Pk[QB][KS]);
  w[P] = cvtpk(c.S[X][KS >> 1][QB][8 * (KS & 1) + 2 * P], c.S[X][KS >> 1][QB][8 * (KS & 1) + 2 * P + 1]);
  c.Pk[QB][KS] = __builtin_bit_cast(bf16x8, w);
}
template <int X, int J> DI void rowsum1(W4C& c) {
  constexpr int qb = J >> 4, ks = (J >> 2) & 3, p = J & 3;
  c.rs0[qb] = fadd_s(c.rs0[qb], c.S[X][ks >> 1][qb][8 * (ks & 1) + 2 * p]); c.rs1[qb] = fadd_s(c.rs1[qb], c.S[X][ks >> 1][qb][8 * (ks & 1) + 2 * p + 1]);
}

template <int X, bool FIRST, bool DMA, int M> DI void gapA(W4C& c, unsigned vslot, const int t) {
  constexpr int Xp = X ^ 1;
  qk_mfma<X, FIRST, M>(c);
  if constexpr (DMA && M >= 24 && (M & 1) == 0) {
    constexpr int q = (M - 24) >> 1;
    const unsigned tn = (unsigned)(t + 2);
    asm volatile("s_mov_b32 m0, %0\n\ts_nop 0\n\tbuffer_load_dwordx4 %1, %2, %3 offen nt lds" :: "s"(c.m0v + tslot(tn) + 1024u * q), "v"(c.vvsrc), "s"(c.srdV),
                 "s"(16384u * tn + c.sofv + 2048u * (q >> 1) + 128u * (q & 1)) : "m0", "memory");
  }
  if constexpr (!FIRST && M == 8) asm volatile("" :: "v"(c.NEGM[0]), "v"(c.NEGM[1]));
  if constexpr (!FIRST) {
    constexpr int base = M + (M >= 4) + (M >= 8) + (M >= 12) + (M >= 16) + (M >= 20) + (M >= 24);
    if constexpr (M <= 25) tr_read<base>(c, vslot);
    if constexpr (M == 3 || M == 7 || M == 11 || M == 15 || M == 19 || M == 23) tr_read<base + 1>(c, vslot);
    if constexpr (M < 16) {
      constexpr int e0 = 2 * M, e1 = 2 * M + 1;
      c.S[Xp][1][e0 >> 4][e0 & 15] = __builtin_amdgcn_exp2f(c.S[Xp][1][e0 >> 4][e0 & 15]);
      c.S[Xp][1][e1 >> 4][e1 & 15] = __builtin_amdgcn_exp2f(c.S[Xp][1][e1 >> 4][e1 & 15]);
      pack1<Xp, (M >> 3), ((M >> 2) & 1), (M & 3)>(c);
    } else {
      constexpr int cc = M - 16;
      pack1<Xp, (cc >> 3), 2 + ((cc >> 2) & 1), (cc & 3)>(c);
      rowsum1<Xp, 16 * (cc >> 3) + 8 + 4 * ((cc >> 2) & 1) + (cc & 3)>(c);
    }
  }
  GAP();
}
#define W4_VLO "+v"(c.vlo[0][0]), "+v"(c.vlo[0][1]), "+v"(c.vlo[0][2]), "+v"(c.vlo[0][3]), "+v"(c.vlo[1][0]), "+v"(c.vlo[1][1]), "+v"(c.vlo[1][2]), "+v"(c.vlo[1][3]), \
               "+v"(c.vlo[2][0]), "+v"(c.vlo[2][1]), "+v"(c.vlo[2][2]), "+v"(c.vlo[2][3]), "+v"(c.vlo[3][0]), "+v"(c.vlo[3][1]), "+v"(c.vlo[3][2]), "+v"(c.vlo[3][3])
#define W4_VUP "+v"(c.vup[0][0]), "+v"(c.vup[0][1]), "+v"(c.vup[0][2]), "+v"(c.vup[0][3]), "+v"(c.vup[1][0]), "+v"(c.vup[1][1]), "+v"(c.vup[1][2]), "+v"(c.vup[1][3]), \
               "+v"(c.vup[2][0]), "+v"(c.vup[2][1]), "+v"(c.vup[2][2]), "+v"(c.vup[2][3]), "+v"(c.vup[3][0]), "+v"(c.vup[3][1]), "+v"(c.vup[3][2]), "+v"(c.vup[3][3])
template <int X, bool FIRST, bool DMA, int VM, int... Ms> DI void phaseA_(W4C& c, const int t, iseq<Ms...>) {
  const unsigned vslot = tslot((unsigned)(t + 4));
  (gapA<X, FIRST, DMA, Ms>(c, vslot, t), ...);
  if constexpr (!FIRST) {
    #pragma unroll
    for (int qb = 0; qb < 2; ++qb) {
      const float rs = fadd_s(c.rs0[qb], c.rs1[qb]);
      auto pr = __builtin_amdgcn_permlane32_swap(__float_as_uint(rs), __float_as_uint(rs), false, false);
      c.lsum[qb] = fadd_s(c.lsum[qb], fadd_s(__uint_as_float(pr[0]), __uint_as_float(pr[1])));
    }
  }
  if constexpr (FIRST) asm volatile("s_waitcnt vmcnt(%0) lgkmcnt(0)\n\ts_barrier" :: "i"(VM) : "memory");
  else { asm volatile("s_waitcnt vmcnt(%16) lgkmcnt(0)" : W4_VLO : "i"(VM) : "memory"); asm volatile("s_barrier" : W4_VUP :: "memory"); }
  GAP();
  if constexpr (!FIRST) {
    #pragma unroll
    for (int ks = 0; ks < 4; ++ks)
      #pragma unroll
      for (int db = 0; db < 4; ++db) c.Vf[db][ks] = __builtin_shufflevector(c.vlo[db][ks], c.vup[db][ks], 0, 1, 2, 3, 4, 5, 6, 7);
  }
}
template <int X, bool FIRST, bool DMA, int VM> DI void phaseA(W4C& c, const int t) { phaseA_<X, FIRST, DMA, VM>(c, t, make_iseq<32>{}); }

template <int X, bool PEEL, bool ZERO1, bool DMA, bool KR, int M> DI void gapB(W4C& c, const unsigned kslot_rd, const int t) {
  if constexpr (!PEEL) pv_mfma<M, ZERO1>(c);
  if constexpr (DMA && M < 8 && (M & 1) == 0) {
    constexpr int q = M >> 1;
    const unsigned tn = (unsigned)(t + 3);
    asm volatile("s_mov_b32 m0, %0\n\ts_nop 0\n\tbuffer_load_dwordx4 %1, %2, %3 offen nt lds" :: "s"(c.m0k + tslot(tn) + 1024u * q), "v"(c.vksrc[q & 1]), "s"(c.srdK),
                 "s"(16384u * tn + c.sofk + 1024u * q) : "m0", "memory");
  }
  if constexpr (M >= 1 && M <= 16 && KR) k_read<M - 1>(c, kslot_rd);
  if constexpr (M >= 8 && M < 16) {
    constexpr int i = M - 8, q = i >> 2, r = 4 * (i & 3);
    if constexpr ((i & 3) == 0) { c.pmx0[q] = max3f(c.S[X][0][q][0], c.S[X][0][q][1], c.S[X][1][q][0]); c.pmx1[q] = max3f(c.S[X][0][q][2], c.S[X][0][q][3], c.S[X][1][q][1]);
                                  c.pmx0[q] = max3f(c.pmx0[q], c.S[X][1][q][2], c.S[X][1][q][3]); }
    else                        { c.pmx0[q] = max3f(c.pmx0[q], c.S[X][0][q][r], c.S[X][0][q][r + 1]); c.pmx1[q] = max3f(c.pmx1[q], c.S[X][0][q][r + 2], c.S[X][0][q][r + 3]);
                                  c.pmx0[q] = max3f(c.pmx0[q], c.S[X][1][q][r], c.S[X][1][q][r + 1]); c.pmx1[q] = max3f(c.pmx1[q], c.S[X][1][q][r + 2], c.S[X][1][q][r + 3]); }
  }
  if constexpr (M == 16 || M == 17) {
    constexpr int q = M - 16; const float mm = max2f(c.pmx0[q], c.pmx1[q]);
    auto pr = __builtin_amdgcn_permlane32_swap(__float_as_uint(mm), __float_as_uint(mm), false, false);
    c.rmax[q] = max2f(__uint_as_float(pr[0]), __uint_as_float(pr[1]));
  }
  if constexpr (M == 18) {
    if constexpr (PEEL) {
      #pragma unroll
      for (int q = 0; q < 2; ++q) { c.mhat[q] = c.rmax[q]; c.lsum[q] = 0.f; c.fct[q] = 1.f;
        #pragma unroll
        for (int kb = 0; kb < 2; ++kb) { _Pragma("unroll") for (int e = 0; e < 16; ++e) c.S[X][kb][q][e] = fsub_s(c.S[X][kb][q][e], c.rmax[q]); }
        negm_bcast(c.NEGM[q], w4_aone(), -c.mhat[q]);
      }
    } else {
      const bool grew = (c.rmax[0] > THR) | (c.rmax[1] > THR);
      if (__builtin_expect(__any((int)grew), 0)) {
        #pragma unroll
        for (int q = 0; q < 2; ++q) { const float dl = max2f(c.rmax[q], 0.f); c.mhat[q] = fadd_s(c.mhat[q], dl); c.fct[q] = __builtin_amdgcn_exp2f(-dl);
          #pragma unroll
          for (int kb = 0; kb < 2; ++kb) { _Pragma("unroll") for (int e = 0; e < 16; ++e) c.S[X][kb][q][e] = fsub_s(c.S[X][kb][q][e], dl); }
          negm_bcast(c.NEGM[q], w4_aone(), -c.mhat[q]);
        }
        c.pend = true;
      }
    }
  }
  if constexpr (M == 18) { c.rs0[0] = c.rs0[1] = c.rs1[0] = c.rs1[1] = 0.f; }
  if constexpr (M >= 19) {
    constexpr int g = M - 19, e_lo = (32 * g) / 13, e_hi = (32 * (g + 1)) / 13;
    {
      constexpr int p_lo = g ? (32 * (g - 1)) / 13 : 0, p_hi = g ? (32 * g) / 13 : 0;
      if constexpr (p_lo + 0 < p_hi) { constexpr int e = p_lo + 0; if constexpr (e & 1) c.rs1[e >> 4] = fadd_s(c.rs1[e >> 4], c.S[X][0][e >> 4][e & 15]); else c.rs0[e >> 4] = fadd_s(c.rs0[e >> 4], c.S[X][0][e >> 4][e & 15]); }
      if constexpr (p_lo + 1 < p_hi) { constexpr int e = p_lo + 1; if constexpr (e & 1) c.rs1[e >> 4] = fadd_s(c.rs1[e >> 4], c.S[X][0][e >> 4][e & 15]); else c.rs0[e >> 4] = fadd_s(c.rs0[e >> 4], c.S[X][0][e >> 4][e & 15]); }
      if constexpr (p_lo + 2 < p_hi) { constexpr int e = p_lo + 2; if constexpr (e & 1) c.rs1[e >> 4] = fadd_s(c.rs1[e >> 4], c.S[X][0][e >> 4][e & 15]); else c.rs0[e >> 4] = fadd_s(c.rs0[e >> 4], c.S[X][0][e >> 4][e & 15]); }
    }
    if constexpr (e_lo + 0 < e_hi) c.S[X][0][(e_lo + 0) >> 4][(e_lo + 0) & 15] = __builtin_amdgcn_exp2f(c.S[X][0][(e_lo + 0) >> 4][(e_lo + 0) & 15]);
    if constexpr (e_lo + 1 < e_hi) c.S[X][0][(e_lo + 1) >> 4][(e_lo + 1) & 15] = __builtin_amdgcn_exp2f(c.S[X][0][(e_lo + 1) >> 4][(e_lo + 1) & 15]);
    if constexpr (e_lo + 2 < e_hi) c.S[X][0][(e_lo + 2) >> 4][(e_lo + 2) & 15] = __builtin_amdgcn_exp2f(c.S[X][0][(e_lo + 2) >> 4][(e_lo + 2) & 15]);
  }
  if constexpr (M == 31) asm volatile("" :: "v"(c.S[X][0][0]), "v"(c.S[X][0][1]), "v"(c.NEGM[0]), "v"(c.NEGM[1]), "v"(c.rs0[0]), "v"(c.rs0[1]), "v"(c.rs1[0]), "v"(c.rs1[1]));
  GAP();
}
template <int X, bool PEEL, bool ZERO1, bool DMA, bool KR, int... Ms> DI void phaseB_(W4C& c, const int t, iseq<Ms...>) {
  const unsigned kslot_rd = tslot((unsigned)(t + 1));
  if constexpr (!PEEL) asm volatile("s_nop 1" ::: "memory");
  GAP();
  (gapB<X, PEEL, ZERO1, DMA, KR, Ms>(c, kslot_rd, t), ...);
  {
    #pragma unroll
    for (int e = (32 * 12) / 13; e < 32; ++e) { if (e & 1) c.rs1[e >> 4] = fadd_s(c.rs1[e >> 4], c.S[X][0][e >> 4][e & 15]); else c.rs0[e >> 4] = fadd_s(c.rs0[e >> 4], c.S[X][0][e >> 4][e & 15]); }
  }
  if constexpr (!PEEL) {
    if (__builtin_expect(c.pend, 0)) {
      float t0, t1;
      asm volatile("s_nop 7\n\ts_nop 7\n\t" W4_RESCALE_TEXT "s_nop 1" : "=&v"(t0), "=&v"(t1) : "v"(c.fct[0]), "v"(c.fct[1]) : W4_ACLOB);
      c.lsum[0] = fmul_s(c.lsum[0], c.fct[0]); c.lsum[1] = fmul_s(c.lsum[1], c.fct[1]); c.pend = false;
    }
  }
  asm volatile("s_waitcnt lgkmcnt(0)" ::: "memory");
  GAP();
}
template <int X, bool PEEL, bool ZERO1, bool DMA, bool KR> DI void phaseB(W4C& c, const int t) { phaseB_<X, PEEL, ZERO1, DMA, KR>(c, t, make_iseq<32>{}); }

template <int... Is> DI void tr_reads(W4C& c, unsigned vslot, iseq<Is...>) { (tr_read<Is>(c, vslot), ...); }
template <int X, int... Cs> DI void packs(W4C& c, iseq<Cs...>) { (pack1<X, ((Cs >> 3) & 1), 2 * (Cs >> 4) + ((Cs >> 2) & 1), (Cs & 3)>(c), ...); }
template <int X, int J> DI void rowsum1_padded(W4C& c) { rowsum1<X, J>(c); asm volatile("s_nop 1"); }
template <int X, int... Js> DI void rowsums(W4C& c, iseq<Js...>) { (rowsum1_padded<X, Js>(c), ...); }
struct QC {
  unsigned vxb[4], vwb, vxsrc[2], vwsrc, m0x, m0w, sofx, sofw;
  __amdgpu_buffer_rsrc_t srdX, srdW, srdO;
  unsigned vwo, sofo, rot, wo[32];
  bf16x4 alo[2][4], aup[2][4];
  bf16x8 xb[2][2];
  f32x16 acc[4][2];
};
#define DMA_ASM(M0V, VOFFV, SRD, SOFF) asm volatile("s_mov_b32 m0, %0\n\ts_nop 0\n\tbuffer_load_dwordx4 %1, %2, %3 offen lds" :: "s"(M0V), "v"(VOFFV), "s"(SRD), "s"(SOFF) : "m0", "memory")
#define DMA_ASM_NT(M0V, VOFFV, SRD, SOFF) asm volatile("s_mov_b32 m0, %0\n\ts_nop 0\n\tbuffer_load_dwordx4 %1, %2, %3 offen nt lds" :: "s"(M0V), "v"(VOFFV), "s"(SRD), "s"(SOFF) : "m0", "memory")
template <int CI, int P> DI void q_dma(QC& q) {
  if constexpr (P < 8) DMA_ASM(q.m0x + QBUF(CI) + 1024u * P, q.vxsrc[P & 1], q.srdX, q.sofx + 8192u * P + 128u * ((CI + q.rot) & 7u));
  else { constexpr int pp = P - 8; DMA_ASM(q.m0w + QBUF(CI) + 1024u * pp, q.vwsrc, q.srdW, q.sofw + 524288u * ((CI + q.rot) & 7u) + 65536u * (pp >> 1) + 128u * (pp & 1)); }
}
template <int T, int P> DI void kv_dma(W4C& c) {
  if constexpr (P < 4) DMA_ASM_NT(c.m0k + tslot(T) + 1024u * P, c.vksrc[P & 1], c.srdK, c.sofk + 16384u * T + 1024u * P);
  else { constexpr int pp = P - 4; DMA_ASM_NT(c.m0v + tslot(T) + 1024u * pp, c.vvsrc, c.srdV, c.sofv + 16384u * T + 2048u * (pp >> 1) + 128u * (pp & 1)); }
}
template <int CI, int KS, int R> DI void q_read(QC& q) {
  constexpr int PAR = KS & 1;
  if constexpr (R < 8) {
    constexpr int db = R >> 1;
    if constexpr ((R & 1) == 0) asm volatile("ds_read_b64_tr_b16 %0, %1 offset:%2" : "=v"(q.alo[PAR][db]) : "v"(q.vwb + QBUF(CI)), "i"(4096 * KS + 512 * db) : "memory");
    else                        asm volatile("ds_read_b64_tr_b16 %0, %1 offset:%2" : "=v"(q.aup[PAR][db]) : "v"(q.vwb + QBUF(CI)), "i"(4096 * KS + 512 * db + 256) : "memory");
  } else {
    constexpr int qb = R - 8;
    asm volatile("ds_read_b128 %0, %1 offset:%2" : "=v"(q.xb[PAR][qb]) : "v"(q.vxb[KS] + QBUF(CI)), "i"(4096 * qb) : "memory");
  }
}
template <int CI, int KS, int M> DI void q_mfma(QC& q) {
  constexpr int PAR = KS & 1, db = M >> 1, qb = M & 1;
  const bf16x8 a = __builtin_shufflevector(q.alo[PAR][db], q.aup[PAR][db], 0, 1, 2, 3, 4, 5, 6, 7);
  if constexpr (CI == 0 && KS == 0) asm volatile("v_mfma_f32_32x32x16_bf16 %0, %1, %2, 0" : "=&v"(q.acc[db][qb]) : "v"(a), "v"(q.xb[PAR][qb]));
  else                              asm volatile("v_mfma_f32_32x32x16_bf16 %0, %1, %2, %0" : "+v"(q.acc[db][qb]) : "v"(a), "v"(q.xb[PAR][qb]));
}
#define ROTEXPR (0u)
constexpr int sched_dma(int ci, int ks, int m) {
  const int j = m == 1 ? 0 : (m == 4 ? 1 : (m == 7 ? 2 : -1));
  if (ci == 7 && ks == 3) return (m & 1) ? 1000 + 16 + (m >> 1) : -1;
  if (ci == 0 && ks < 3) return (m & 1) ? 200 + 4 * ks + (m >> 1) : (m == 2 ? 1000 + ks : ((m == 6 && ks == 2) ? 1003 : -1));
  if (j < 0) return -1;
  const int i = ks == 3 ? ci : ci - 1, st = ks == 3 ? 0 : ks + 1, p = 3 * st + j;
  if (i < 0) return -1;
  if (i + 3 < 8) return 100 * (i + 3) + p;
  if (i == 5) return p < 4 ? 1000 + 4 + p : -1;
  if (i == 6) return p < 8 ? 1000 + 8 + p : -1;
  return -1;
}
constexpr int sched_wo(int ci, int ks, int m) { return (ci >= 1 && ci <= 4 && (m == 2 || m == 5)) ? ((ci - 1) * 4 + ks) * 2 + (m == 5) : -1; }
constexpr int sched_vm(int ci) {
  int n = 0;
  for (int c = 0; c < 2; ++c) for (int p = 0; p < 12; ++p) { n = (c == ci + 1) ? 0 : n + 1; }
  for (int c = 0; c <= ci; ++c) for (int ks = 0; ks < 4; ++ks) { if (c == ci && ks == 3) break;
    for (int m = 0; m < 8; ++m) {
      const int d = sched_dma(c, ks, m);
      if (d >= 0) n = ((d < 1000 && d / 100 == ci + 1) || (ci == 7 && d >= 1000 && d < 1004)) ? 0 : n + 1;
      if (sched_wo(c, ks, m) >= 0) n = (ci == 7) ? 0 : n + 1;
    } }
  return n;
}
template <int I> DI void wo_load(QC& q) {
  constexpr int i = I >> 3, j = I & 7;
  asm volatile("buffer_load_ushort %0, %1, %2, %3 offen offset:%4" : "=v"(q.wo[I]) : "v"(q.vwo), "s"(q.srdO), "s"(q.sofo + 8192u * (j >> 2)), "i"(1024 * (j & 3) + 64 * i) : "memory");
}
template <int CI, int KS, int M> DI void q_gap(QC& q, W4C& c) {
  q_mfma<CI, KS, M>(q);
  constexpr int NCI = KS == 3 ? CI + 1 : CI, NKS = (KS + 1) & 3;
  if constexpr (NCI < 8 && M < 5) { q_read<NCI, NKS, 2 * M>(q); q_read<NCI, NKS, 2 * M + 1>(q); }
  constexpr int D = sched_dma(CI, KS, M), W = sched_wo(CI, KS, M);
  if constexpr (D >= 1000) kv_dma<((D - 1000) >> 3), ((D - 1000) & 7)>(c);
  else if constexpr (D >= 0) q_dma<(D / 100), (D % 100)>(q);
  if constexpr (W >= 0) wo_load<W>(q);
  if constexpr (KS == 3 && CI == 7) { k_read<2 * M>(c, tslot(0)); k_read<2 * M + 1>(c, tslot(0)); }
  GAP();
}
template <int CI, int KS, int... Ms> DI void q_step_(QC& q, W4C& c, iseq<Ms...>) {
  constexpr int PAR = KS & 1;
  if constexpr (KS == 3) {
    constexpr int VM = sched_vm(CI);
    asm volatile("s_waitcnt vmcnt(%10) lgkmcnt(0)\n\ts_barrier" : "+v"(q.alo[PAR][0]), "+v"(q.alo[PAR][1]), "+v"(q.alo[PAR][2]), "+v"(q.alo[PAR][3]),
                 "+v"(q.aup[PAR][0]), "+v"(q.aup[PAR][1]), "+v"(q.aup[PAR][2]), "+v"(q.aup[PAR][3]), "+v"(q.xb[PAR][0]), "+v"(q.xb[PAR][1]) : "i"(VM) : "memory");
  } else {
    asm volatile("s_waitcnt lgkmcnt(0)" : "+v"(q.alo[PAR][0]), "+v"(q.alo[PAR][1]), "+v"(q.alo[PAR][2]), "+v"(q.alo[PAR][3]),
                 "+v"(q.aup[PAR][0]), "+v"(q.aup[PAR][1]), "+v"(q.aup[PAR][2]), "+v"(q.aup[PAR][3]), "+v"(q.xb[PAR][0]), "+v"(q.xb[PAR][1]) :: "memory");
  }
  GAP();
  (q_gap<CI, KS, Ms>(q, c), ...);
}
template <int CI> DI void q_chunk(QC& q, W4C& c) {
  q_step_<CI, 0>(q, c, make_iseq<8>{}); q_step_<CI, 1>(q, c, make_iseq<8>{}); q_step_<CI, 2>(q, c, make_iseq<8>{}); q_step_<CI, 3>(q, c, make_iseq<8>{});
}
template <int... Cs> DI void q_chunks(QC& q, W4C& c, iseq<Cs...>) { (q_chunk<Cs>(q, c), ...); }
template <int CI, int... Ps> DI void q_dmas(QC& q, iseq<Ps...>) { (q_dma<CI, Ps>(q), ...); }
template <int... Rs> DI void q_reads0(QC& q, iseq<Rs...>) { (q_read<0, 0, Rs>(q), ...); }

template <int BASE> DI void acc_read8(float (&o)[8]) {
  asm volatile("v_accvgpr_read_b32 %0, a[%c8]\n\tv_accvgpr_read_b32 %1, a[%c9]\n\tv_accvgpr_read_b32 %2, a[%c10]\n\tv_accvgpr_read_b32 %3, a[%c11]\n\t"
               "v_accvgpr_read_b32 %4, a[%c12]\n\tv_accvgpr_read_b32 %5, a[%c13]\n\tv_accvgpr_read_b32 %6, a[%c14]\n\tv_accvgpr_read_b32 %7, a[%c15]"
               : "=v"(o[0]), "=v"(o[1]), "=v"(o[2]), "=v"(o[3]), "=v"(o[4]), "=v"(o[5]), "=v"(o[6]), "=v"(o[7])
               : "i"(BASE), "i"(BASE + 1), "i"(BASE + 2), "i"(BASE + 3), "i"(BASE + 4), "i"(BASE + 5), "i"(BASE + 6), "i"(BASE + 7));
}
template <int AQ> DI void acc_write4(const u32x4& w) {
  asm volatile("v_accvgpr_write_b32 a[%c4], %0\n\tv_accvgpr_write_b32 a[%c5], %1\n\tv_accvgpr_write_b32 a[%c6], %2\n\tv_accvgpr_write_b32 a[%c7], %3"
               :: "v"(w[0]), "v"(w[1]), "v"(w[2]), "v"(w[3]), "i"(AQ), "i"(AQ + 1), "i"(AQ + 2), "i"(AQ + 3) : W4_ACLOB_HI);
}
template <int I> DI void q_convert(QC& q, float C) {
  constexpr int qb = I & 1, d0 = I >> 1, db = d0 >> 1, s = d0 & 1;
  u32x4 w;
  #pragma unroll
  for (int e = 0; e < 4; ++e) w[e] = cvtpk(fmul_s(q.acc[db][qb][8 * s + 2 * e], C), fmul_s(q.acc[db][qb][8 * s + 2 * e + 1], C));
  acc_write4<AQ_(qb, d0)>(w);
}
template <int M> DI void gapA0(W4C& c, QC& q, float C) {
  qk_mfma<0, true, M>(c);
  GAP();
  if constexpr (M < 28 && (M & 3) < 2) { q_convert<2 * ((M >> 2) + 1) + (M & 3)>(q, C); asm volatile("s_nop 1" ::: "memory"); }
  if constexpr (M >= 24 && (M & 1) == 0) kv_dma<2, 4 + ((M - 24) >> 1)>(c);
  GAP();
}
template <int... Ms> DI void phaseA0(W4C& c, QC& q, float C, iseq<Ms...>) {
  (gapA0<Ms>(c, q, C), ...);
  asm volatile("s_waitcnt vmcnt(16) lgkmcnt(0)\n\ts_barrier" ::: "memory");
  GAP();
}
template <int I> DI void o_store(const float (&il)[2], char* obase, unsigned voff) {
  constexpr int qb = I >> 3, kk = I & 7, db = kk >> 1, s = kk & 1;
  float o[8]; acc_read8<AO_(db, qb) + 8 * s>(o);
  u32x4 w;
  #pragma unroll
  for (int e = 0; e < 4; ++e) w[e] = cvtpk(fmul_s(o[2 * e], il[qb]), fmul_s(o[2 * e + 1], il[qb]));
  *(u32x4*)(obase + (size_t)qb * (256 * 1024) + kk * 1024 + voff) = w;
}
template <int J, int KS> DI void pv_last(W4C& c) {
  constexpr int db = J >> 1, qb = J & 1, ao = AO_(db, qb);
  asm volatile("v_mfma_f32_32x32x16_bf16 a[%c2:%c3], %0, %1, a[%c2:%c3]" :: "v"(c.Vf[db][KS]), "v"(c.Pk[qb][KS]), "i"(ao), "i"(ao + 15) : W4_ACLOB);
}
template <int J> DI void drain_acc(W4C& c, const float (&il)[2], char* obase, unsigned voff) {
  constexpr int Jp = J - 1;
  pv_last<J, 0>(c); GAP();
  pv_last<J, 1>(c); GAP();
  pv_last<J, 2>(c); GAP();
  if constexpr (J > 0) { asm volatile("s_nop 7\n\ts_nop 1" ::: "memory"); o_store<8 * (Jp & 1) + 2 * (Jp >> 1)>(il, obase, voff); }
  GAP();
  pv_last<J, 3>(c); GAP();
  if constexpr (J > 0) o_store<8 * (Jp & 1) + 2 * (Jp >> 1) + 1>(il, obase, voff);
  GAP();
}
template <int... Js> DI void drain_accs(W4C& c, const float (&il)[2], char* obase, unsigned voff, iseq<Js...>) { (drain_acc<Js>(c, il, obase, voff), ...); }

constexpr size_t WS_OF = 0, WS_WOF = (size_t)16 << 20;

extern "C" __global__ __launch_bounds__(256, 1)
void attn_fused(const unsigned short* __restrict__ X, const unsigned short* __restrict__ Kc, const unsigned short* __restrict__ Vc,
                const unsigned short* __restrict__ Wq, const unsigned short* __restrict__ Wo, char* __restrict__ ws) {
  extern __shared__ __attribute__((aligned(16))) char lds[];
  const unsigned tid = threadIdx.x, lane = tid & 63u, wid = tid >> 6, l32 = lane & 31u, hi = lane >> 5;
  const unsigned g = blockIdx.x, hd = 4u * (g & 7u) + ((g >> 3) & 3u), b = g >> 5;
  const float C = 0.12751743082f;
  STAMP(0);
  W4C c; QC q;
  c.ldsb = (unsigned)(size_t)lds;
  #pragma unroll
  for (int j = 0; j < 4; ++j) c.vkb[j] = c.ldsb + KOFF + 256u * l32 + 16u * ((2u * j + hi) ^ (l32 & 7u));
  c.vvb = c.ldsb + (((lane & 3u) << 3) | (((lane >> 2) & 3u) << 6) | (((lane >> 4) & 1u) << 5) | (hi << 8));
  #pragma unroll
  for (int par = 0; par < 2; ++par) c.vksrc[par] = 256u * (lane >> 4) + 16u * ((lane & 15u) ^ (4u * par + (lane >> 4)));
  c.vvsrc = 256u * ((lane >> 2) & 7u) + 64u * hi + 16u * (lane & 3u);
  c.m0k = rfl(c.ldsb + KOFF + 4096u * wid); c.m0v = rfl(c.ldsb + 4096u * wid);
  c.sofk = rfl(4096u * wid); c.sofv = rfl(4096u * wid);
  const size_t kvoff = ((size_t)b * NH + hd) * (SK * DH * 2);
  c.srdK = __builtin_amdgcn_make_buffer_rsrc((void*)((const char*)Kc + kvoff), (short)0, SK * DH * 2, SRD_WORD3);
  c.srdV = __builtin_amdgcn_make_buffer_rsrc((void*)((const char*)Vc + kvoff), (short)0, SK * DH * 2, SRD_WORD3);
  #pragma unroll
  for (int ks = 0; ks < 4; ++ks) q.vxb[ks] = c.ldsb + 8192u * wid + 128u * l32 + 16u * ((2u * ks + hi) ^ ((l32 >> 1) & 7u));
  { const unsigned p = lane & 3u, sp = ((p & 1u) << 1) | (p >> 1);
    q.vwb = c.ldsb + QWOFF + ((hi << 11) | (((lane >> 2) & 3u) << 6) | (((lane >> 4) & 1u) << 5) | (sp << 3)); }
  #pragma unroll
  for (int par = 0; par < 2; ++par) q.vxsrc[par] = 1024u * (lane >> 3) + 16u * ((lane & 7u) ^ (4u * par + (lane >> 4)));
  q.vwsrc = 8192u * ((lane >> 2) & 7u) + 64u * hi + 16u * (lane & 3u);
  q.m0x = rfl(c.ldsb + 8192u * wid); q.m0w = rfl(c.ldsb + QWOFF + 4096u * wid);
  q.sofx = rfl(65536u * wid); q.sofw = rfl(131072u * wid);
  q.srdX = __builtin_amdgcn_make_buffer_rsrc((void*)((const char*)X + (size_t)b * (SQ * DM * 2)), (short)0, SQ * DM * 2, SRD_WORD3);
  q.srdW = __builtin_amdgcn_make_buffer_rsrc((void*)((const char*)Wq + hd * 256u), (short)0, DM * NH * DH * 2 - (int)hd * 256, SRD_WORD3);
  q.srdO = __builtin_amdgcn_make_buffer_rsrc((void*)Wo, (short)0, NH * DH * DM * 2, SRD_WORD3);
  q.rot = ROTEXPR;
  q.vwo = 4096u * hi + 2u * l32; q.sofo = rfl(16384u * g + 256u * wid);
  asm volatile("s_nop 4" ::: "memory");
  q_dmas<0>(q, make_iseq<12>{});
  q_dmas<1>(q, make_iseq<12>{});
  STAMP(1);
  asm volatile("s_waitcnt vmcnt(12)\n\ts_barrier" ::: "memory");
  STAMP(2);
  q_reads0(q, make_iseq<10>{});
  q_chunks(q, c, make_iseq<8>{});
  STAMP(3);
  asm volatile("" : "+v"(q.wo[0]), "+v"(q.wo[1]), "+v"(q.wo[2]), "+v"(q.wo[3]), "+v"(q.wo[4]), "+v"(q.wo[5]), "+v"(q.wo[6]), "+v"(q.wo[7]),
                    "+v"(q.wo[8]), "+v"(q.wo[9]), "+v"(q.wo[10]), "+v"(q.wo[11]), "+v"(q.wo[12]), "+v"(q.wo[13]), "+v"(q.wo[14]), "+v"(q.wo[15]));
  asm volatile("" : "+v"(q.wo[16]), "+v"(q.wo[17]), "+v"(q.wo[18]), "+v"(q.wo[19]), "+v"(q.wo[20]), "+v"(q.wo[21]), "+v"(q.wo[22]), "+v"(q.wo[23]),
                    "+v"(q.wo[24]), "+v"(q.wo[25]), "+v"(q.wo[26]), "+v"(q.wo[27]), "+v"(q.wo[28]), "+v"(q.wo[29]), "+v"(q.wo[30]), "+v"(q.wo[31]));
  #pragma unroll
  for (int i = 0; i < 4; ++i) {
    u32x4 w;
    #pragma unroll
    for (int e = 0; e < 4; ++e) w[e] = q.wo[8 * i + 2 * e] | (q.wo[8 * i + 2 * e + 1] << 16);
    *(u32x4*)(ws + WS_WOF + ((size_t)(4u * wid + i) * 256 + g) * 1024 + lane * 16u) = w;
  }
  asm volatile("s_nop 7\n\ts_nop 7" : "+v"(q.acc[0][0]), "+v"(q.acc[0][1]), "+v"(q.acc[1][0]), "+v"(q.acc[1][1]), "+v"(q.acc[2][0]), "+v"(q.acc[2][1]), "+v"(q.acc[3][0]), "+v"(q.acc[3][1]) :: "memory");
  q_convert<0>(q, C); q_convert<1>(q, C);
  asm volatile("s_waitcnt lgkmcnt(0)\n\ts_nop 1" ::: "memory");

  STAMP(4);
  c.mhat[0] = c.mhat[1] = 0.f; c.lsum[0] = c.lsum[1] = 0.f; c.fct[0] = c.fct[1] = 1.f; c.pend = false;
  phaseA0(c, q, C, make_iseq<32>{});  phaseB<0, true,  false, true, true>(c, 0);
  phaseA<1, false, true, 16>(c, 1);  phaseB<1, false, true,  true,  true>(c, 1);
  STAMP(5);
  for (int t = 2; t < 12; t += 2) {
    phaseA<0, false, true, 12>(c, t);     phaseB<0, false, false, true, true>(c, t);
    phaseA<1, false, true, 12>(c, t + 1); phaseB<1, false, false, true, true>(c, t + 1);
  }
  STAMP(6);
  phaseA<0, false, true, 12>(c, 12);  phaseB<0, false, false, true,  true>(c, 12);
  phaseA<1, false, true, 12>(c, 13);  phaseB<1, false, false, false, true>(c, 13);
  phaseA<0, false, false, 4>(c, 14);  phaseB<0, false, false, false, true>(c, 14);
  phaseA<1, false, false, 0>(c, 15);  phaseB<1, false, false, false, false>(c, 15);
  STAMP(7);
  {
    const unsigned vslot = tslot(NT - 1);
    tr_reads(c, vslot, make_iseq<32>{});
    #pragma unroll
    for (int e = 0; e < 32; ++e) c.S[1][1][e >> 4][e & 15] = __builtin_amdgcn_exp2f(c.S[1][1][e >> 4][e & 15]);
    packs<1>(c, make_iseq<32>{});
    rowsums<1>(c, iseq<8, 9, 10, 11, 12, 13, 14, 15, 24, 25, 26, 27, 28, 29, 30, 31>{});
    #pragma unroll
    for (int qb = 0; qb < 2; ++qb) { const float rs = fadd_s(c.rs0[qb], c.rs1[qb]);
      auto pr = __builtin_amdgcn_permlane32_swap(__float_as_uint(rs), __float_as_uint(rs), false, false);
      c.lsum[qb] = fadd_s(c.lsum[qb], fadd_s(__uint_as_float(pr[0]), __uint_as_float(pr[1]))); }
    asm volatile("s_waitcnt lgkmcnt(0)" : W4_VLO :: "memory"); asm volatile("" : W4_VUP :: "memory");
    GAP();
    #pragma unroll
    for (int ks = 0; ks < 4; ++ks)
      #pragma unroll
      for (int db = 0; db < 4; ++db) c.Vf[db][ks] = __builtin_shufflevector(c.vlo[db][ks], c.vup[db][ks], 0, 1, 2, 3, 4, 5, 6, 7);
    asm volatile("s_nop 1" ::: "memory");
    float il[2];
    il[0] = __builtin_amdgcn_rcpf(c.lsum[0]); il[1] = __builtin_amdgcn_rcpf(c.lsum[1]);
    asm volatile("s_nop 1" : "+v"(il[0]), "+v"(il[1]));
    char* obase = ws + WS_OF + ((size_t)(b * 8u + 2u * wid) * 256 + hd * 8u) * 1024;
    drain_accs(c, il, obase, lane * 16u, make_iseq<8>{});
    STAMP(8);
    asm volatile("s_nop 7\n\ts_nop 7" ::: "memory");
    o_store<8 + 6>(il, obase, lane * 16u); o_store<8 + 7>(il, obase, lane * 16u);
  }
}

extern "C" __global__ __launch_bounds__(512, 2)
void oproj(const char* __restrict__ ws, unsigned short* __restrict__ out) {
  __shared__ __attribute__((aligned(16))) float red[8 * 64 * 68];
  const unsigned tid = threadIdx.x, lane = tid & 63u, wid = tid >> 6, l32 = lane & 31u, hi = lane >> 5;
  const unsigned g = blockIdx.x, xc = g & 7u, idx = g >> 3;
  const unsigned qt = 4u * xc + (idx & 3u), nt = idx >> 2;
  const char* ofp = ws + WS_OF + ((size_t)(2u * qt) * 256 + 32u * wid) * 1024 + lane * 16u;
  const char* wfp = ws + WS_WOF + ((size_t)(2u * nt) * 256 + 32u * wid) * 1024 + lane * 16u;
  f32x16 acc[2][2] = {};
  bf16x8 fo[4][2], fw[4][2];
  #pragma unroll
  for (int i = 0; i < 4; ++i) {
    fo[i][0] = *(const bf16x8*)(ofp + i * 1024); fo[i][1] = *(const bf16x8*)(ofp + 262144 + i * 1024);
    fw[i][0] = *(const bf16x8*)(wfp + i * 1024); fw[i][1] = *(const bf16x8*)(wfp + 262144 + i * 1024);
  }
  #pragma unroll 1
  for (int k0 = 0; k0 < 32; k0 += 4) {
    #pragma unroll
    for (int i = 0; i < 4; ++i) {
      const bf16x8 o0 = fo[i][0], o1 = fo[i][1], w0 = fw[i][0], w1 = fw[i][1];
      if (k0 + 4 < 32) {
        const int kn = k0 + 4 + i;
        fo[i][0] = *(const bf16x8*)(ofp + kn * 1024); fo[i][1] = *(const bf16x8*)(ofp + 262144 + kn * 1024);
        fw[i][0] = *(const bf16x8*)(wfp + kn * 1024); fw[i][1] = *(const bf16x8*)(wfp + 262144 + kn * 1024);
      }
      acc[0][0] = __builtin_amdgcn_mfma_f32_32x32x16_bf16(w0, o0, acc[0][0], 0, 0, 0);
      acc[0][1] = __builtin_amdgcn_mfma_f32_32x32x16_bf16(w0, o1, acc[0][1], 0, 0, 0);
      acc[1][0] = __builtin_amdgcn_mfma_f32_32x32x16_bf16(w1, o0, acc[1][0], 0, 0, 0);
      acc[1][1] = __builtin_amdgcn_mfma_f32_32x32x16_bf16(w1, o1, acc[1][1], 0, 0, 0);
    }
  }
  float* rw = red + wid * (64 * 68);
  #pragma unroll
  for (int nb = 0; nb < 2; ++nb)
    #pragma unroll
    for (int qb = 0; qb < 2; ++qb)
      #pragma unroll
      for (int gq = 0; gq < 4; ++gq) {
        f32x4 v = {acc[nb][qb][4 * gq], acc[nb][qb][4 * gq + 1], acc[nb][qb][4 * gq + 2], acc[nb][qb][4 * gq + 3]};
        *(f32x4*)(rw + (32 * qb + l32) * 68 + 32 * nb + 8 * gq + 4 * hi) = v;
      }
  __syncthreads();
  {
    const unsigned qr = tid >> 3, n0 = (tid & 7u) * 8u;
    f32x4 s0 = {}, s1 = {};
    #pragma unroll
    for (int w = 0; w < 8; ++w) {
      s0 += *(const f32x4*)(red + w * (64 * 68) + qr * 68 + n0);
      s1 += *(const f32x4*)(red + w * (64 * 68) + qr * 68 + n0 + 4);
    }
    u32x4 w; w[0] = cvtpk(s0[0], s0[1]); w[1] = cvtpk(s0[2], s0[3]); w[2] = cvtpk(s1[0], s1[1]); w[3] = cvtpk(s1[2], s1[3]);
    *(u32x4*)(out + (size_t)(64u * qt + qr) * DM + 64u * nt + n0) = w;
  }
}

extern "C" void kernel_launch(void* const* d_in, const int* in_sizes, int n_in, void* d_out, int out_size, void* d_ws, size_t ws_size, hipStream_t stream) {
  (void)in_sizes; (void)n_in; (void)out_size; (void)ws_size;
  (void)hipFuncSetAttribute((const void*)attn_fused, hipFuncAttributeMaxDynamicSharedMemorySize, (int)SHM_ATTN);
  hipLaunchKernelGGL(attn_fused, dim3(256), dim3(256), SHM_ATTN, stream, (const unsigned short*)d_in[0], (const unsigned short*)d_in[1], (const unsigned short*)d_in[2],
                     (const unsigned short*)d_in[3], (const unsigned short*)d_in[4], (char*)d_ws);
  hipLaunchKernelGGL(oproj, dim3(256), dim3(512), 0, stream, (const char*)d_ws, (unsigned short*)d_out);
}
```

```cpp
#include <hip/hip_runtime.h>
#include <utility>

constexpr int NB = 8, SQ = 256, DM = 512, NH = 32, SK = 1024, DH = 128, NT = SK / 64;
constexpr float THR = 8.0f;
constexpr size_t SHM_ATTN = 163840;
typedef float    f32x16 __attribute__((ext_vector_type(16)));
typedef float    f32x2  __attribute__((ext_vector_type(2)));
typedef float    f32x4  __attribute__((ext_vector_type(4)));
typedef __bf16   bf16x8 __attribute__((ext_vector_type(8)));
typedef __bf16   bf16x4 __attribute__((ext_vector_type(4)));
typedef __bf16   bf16x2 __attribute__((ext_vector_type(2)));
typedef unsigned u32x4  __attribute__((ext_vector_type(4)));
typedef unsigned u32x2  __attribute__((ext_vector_type(2)));
template <int... Is> using iseq = std::integer_sequence<int, Is...>;
template <int N> using make_iseq = std::make_integer_sequence<int, N>;

#define SLOTB 0x8000u
#define KOFF  0x4000u
#define QBUF(i) (49152u * ((i) % 3))
__device__ __forceinline__ constexpr unsigned tslot(unsigned t) { return ((t + 4u) % 5u) * SLOTB; }
#define QWOFF 32768u
#define SRD_WORD3 0x00020000
#define ST_AUX 16
#define DI __device__ __forceinline__
#define GAP() __builtin_amdgcn_sched_barrier(0)
#define W4_ACLOB "a0","a1","a2","a3","a4","a5","a6","a7","a8","a9","a10","a11","a12","a13","a14","a15","a16","a17","a18","a19","a20","a21","a22","a23","a24","a25","a26","a27","a28","a29","a30","a31","a32","a33","a34","a35","a36","a37","a38","a39","a40","a41","a42","a43","a44","a45","a46","a47","a48","a49","a50","a51","a52","a53","a54","a55","a56","a57","a58","a59","a60","a61","a62","a63","a64","a65","a66","a67","a68","a69","a70","a71","a72","a73","a74","a75","a76","a77","a78","a79","a80","a81","a82","a83","a84","a85","a86","a87","a88","a89","a90","a91","a92","a93","a94","a95","a96","a97","a98","a99","a100","a101","a102","a103","a104","a105","a106","a107","a108","a109","a110","a111","a112","a113","a114","a115","a116","a117","a118","a119","a120","a121","a122","a123","a124","a125","a126","a127"
#define W4_ACLOB_HI "a128","a129","a130","a131","a132","a133","a134","a135","a136","a137","a138","a139","a140","a141","a142","a143","a144","a145","a146","a147","a148","a149","a150","a151","a152","a153","a154","a155","a156","a157","a158","a159","a160","a161","a162","a163","a164","a165","a166","a167","a168","a169","a170","a171","a172","a173","a174","a175","a176","a177","a178","a179","a180","a181","a182","a183","a184","a185","a186","a187","a188","a189","a190","a191","a192","a193","a194","a195","a196","a197","a198","a199","a200","a201","a202","a203","a204","a205","a206","a207","a208","a209","a210","a211","a212","a213","a214","a215","a216","a217","a218","a219","a220","a221","a222","a223","a224","a225","a226","a227","a228","a229","a230","a231","a232","a233","a234","a235","a236","a237","a238","a239","a240","a241","a242","a243","a244","a245","a246","a247","a248","a249","a250","a251","a252","a253","a254","a255"
#define W4_RESCALE_TEXT "v_accvgpr_read_b32 %0, a0\n\tv_mul_f32 %0, %2, %0\n\tv_accvgpr_write_b32 a0, %0\n\t" "v_accvgpr_read_b32 %1, a1\n\tv_mul_f32 %1, %2, %1\n\tv_accvgpr_write_b32 a1, %1\n\t" "v_accvgpr_read_b32 %0, a2\n\tv_mul_f32 %0, %2, %0\n\tv_accvgpr_write_b32 a2, %0\n\t" "v_accvgpr_read_b32 %1, a3\n\tv_mul_f32 %1, %2, %1\n\tv_accvgpr_write_b32 a3, %1\n\t" "v_accvgpr_read_b32 %0, a4\n\tv_mul_f32 %0, %2, %0\n\tv_accvgpr_write_b32 a4, %0\n\t" "v_accvgpr_read_b32 %1, a5\n\tv_mul_f32 %1, %2, %1\n\tv_accvgpr_write_b32 a5, %1\n\t" "v_accvgpr_read_b32 %0, a6\n\tv_mul_f32 %0, %2, %0\n\tv_accvgpr_write_b32 a6, %0\n\t" "v_accvgpr_read_b32 %1, a7\n\tv_mul_f32 %1, %2, %1\n\tv_accvgpr_write_b32 a7, %1\n\t" "v_accvgpr_read_b32 %0, a8\n\tv_mul_f32 %0, %2, %0\n\tv_accvgpr_write_b32 a8, %0\n\t" "v_accvgpr_read_b32 %1, a9\n\tv_mul_f32 %1, %2, %1\n\tv_accvgpr_write_b32 a9, %1\n\t" "v_accvgpr_read_b32 %0, a10\n\tv_mul_f32 %0, %2, %0\n\tv_accvgpr_write_b32 a10, %0\n\t" "v_accvgpr_read_b32 %1, a11\n\tv_mul_f32 %1, %2, %1\n\tv_accvgpr_write_b32 a11, %1\n\t" "v_accvgpr_read_b32 %0, a12\n\tv_mul_f32 %0, %2, %0\n\tv_accvgpr_write_b32 a12, %0\n\t" "v_accvgpr_read_b32 %1, a13\n\tv_mul_f32 %1, %2, %1\n\tv_accvgpr_write_b32 a13, %1\n\t" "v_accvgpr_read_b32 %0, a14\n\tv_mul_f32 %0, %2, %0\n\tv_accvgpr_write_b32 a14, %0\n\t" "v_accvgpr_read_b32 %1, a15\n\tv_mul_f32 %1, %2, %1\n\tv_accvgpr_write_b32 a15, %1\n\t" "v_accvgpr_read_b32 %0, a16\n\tv_mul_f32 %0, %3, %0\n\tv_accvgpr_write_b32 a16, %0\n\t" "v_accvgpr_read_b32 %1, a17\n\tv_mul_f32 %1, %3, %1\n\tv_accvgpr_write_b32 a17, %1\n\t" "v_accvgpr_read_b32 %0, a18\n\tv_mul_f32 %0, %3, %0\n\tv_accvgpr_write_b32 a18, %0\n\t" "v_accvgpr_read_b32 %1, a19\n\tv_mul_f32 %1, %3, %1\n\tv_accvgpr_write_b32 a19, %1\n\t" "v_accvgpr_read_b32 %0, a20\n\tv_mul_f32 %0, %3, %0\n\tv_accvgpr_write_b32 a20, %0\n\t" "v_accvgpr_read_b32 %1, a21\n\tv_mul_f32 %1, %3, %1\n\tv_accvgpr_write_b32 a21, %1\n\t" "v_accvgpr_read_b32 %0, a22\n\tv_mul_f32 %0, %3, %0\n\tv_accvgpr_write_b32 a22, %0\n\t" "v_accvgpr_read_b32 %1, a23\n\tv_mul_f32 %1, %3, %1\n\tv_accvgpr_write_b32 a23, %1\n\t" "v_accvgpr_read_b32 %0, a24\n\tv_mul_f32 %0, %3, %0\n\tv_accvgpr_write_b32 a24, %0\n\t" "v_accvgpr_read_b32 %1, a25\n\tv_mul_f32 %1, %3, %1\n\tv_accvgpr_write_b32 a25, %1\n\t" "v_accvgpr_read_b32 %0, a26\n\tv_mul_f32 %0, %3, %0\n\tv_accvgpr_write_b32 a26, %0\n\t" "v_accvgpr_read_b32 %1, a27\n\tv_mul_f32 %1, %3, %1\n\tv_accvgpr_write_b32 a27, %1\n\t" "v_accvgpr_read_b32 %0, a28\n\tv_mul_f32 %0, %3, %0\n\tv_accvgpr_write_b32 a28, %0\n\t" "v_accvgpr_read_b32 %1, a29\n\tv_mul_f32 %1, %3, %1\n\tv_accvgpr_write_b32 a29, %1\n\t" "v_accvgpr_read_b32 %0, a30\n\tv_mul_f32 %0, %3, %0\n\tv_accvgpr_write_b32 a30, %0\n\t" "v_accvgpr_read_b32 %1, a31\n\tv_mul_f32 %1, %3, %1\n\tv_accvgpr_write_b32 a31, %1\n\t" "v_accvgpr_read_b32 %0, a32\n\tv_mul_f32 %0, %2, %0\n\tv_accvgpr_write_b32 a32, %0\n\t" "v_accvgpr_read_b32 %1, a33\n\tv_mul_f32 %1, %2, %1\n\tv_accvgpr_write_b32 a33, %1\n\t" "v_accvgpr_read_b32 %0, a34\n\tv_mul_f32 %0, %2, %0\n\tv_accvgpr_write_b32 a34, %0\n\t" "v_accvgpr_read_b32 %1, a35\n\tv_mul_f32 %1, %2, %1\n\tv_accvgpr_write_b32 a35, %1\n\t" "v_accvgpr_read_b32 %0, a36\n\tv_mul_f32 %0, %2, %0\n\tv_accvgpr_write_b32 a36, %0\n\t" "v_accvgpr_read_b32 %1, a37\n\tv_mul_f32 %1, %2, %1\n\tv_accvgpr_write_b32 a37, %1\n\t" "v_accvgpr_read_b32 %0, a38\n\tv_mul_f32 %0, %2, %0\n\tv_accvgpr_write_b32 a38, %0\n\t" "v_accvgpr_read_b32 %1, a39\n\tv_mul_f32 %1, %2, %1\n\tv_accvgpr_write_b32 a39, %1\n\t" "v_accvgpr_read_b32 %0, a40\n\tv_mul_f32 %0, %2, %0\n\tv_accvgpr_write_b32 a40, %0\n\t" "v_accvgpr_read_b32 %1, a41\n\tv_mul_f32 %1, %2, %1\n\tv_accvgpr_write_b32 a41, %1\n\t" "v_accvgpr_read_b32 %0, a42\n\tv_mul_f32 %0, %2, %0\n\tv_accvgpr_write_b32 a42, %0\n\t" "v_accvgpr_read_b32 %1, a43\n\tv_mul_f32 %1, %2, %1\n\tv_accvgpr_write_b32 a43, %1\n\t" "v_accvgpr_read_b32 %0, a44\n\tv_mul_f32 %0, %2, %0\n\tv_accvgpr_write_b32 a44, %0\n\t" "v_accvgpr_read_b32 %1, a45\n\tv_mul_f32 %1, %2, %1\n\tv_accvgpr_write_b32 a45, %1\n\t" "v_accvgpr_read_b32 %0, a46\n\tv_mul_f32 %0, %2, %0\n\tv_accvgpr_write_b32 a46, %0\n\t" "v_accvgpr_read_b32 %1, a47\n\tv_mul_f32 %1, %2, %1\n\tv_accvgpr_write_b32 a47, %1\n\t" "v_accvgpr_read_b32 %0, a48\n\tv_mul_f32 %0, %3, %0\n\tv_accvgpr_write_b32 a48, %0\n\t" "v_accvgpr_read_b32 %1, a49\n\tv_mul_f32 %1, %3, %1\n\tv_accvgpr_write_b32 a49, %1\n\t" "v_accvgpr_read_b32 %0, a50\n\tv_mul_f32 %0, %3, %0\n\tv_accvgpr_write_b32 a50, %0\n\t" "v_accvgpr_read_b32 %1, a51\n\tv_mul_f32 %1, %3, %1\n\tv_accvgpr_write_b32 a51, %1\n\t" "v_accvgpr_read_b32 %0, a52\n\tv_mul_f32 %0, %3, %0\n\tv_accvgpr_write_b32 a52, %0\n\t" "v_accvgpr_read_b32 %1, a53\n\tv_mul_f32 %1, %3, %1\n\tv_accvgpr_write_b32 a53, %1\n\t" "v_accvgpr_read_b32 %0, a54\n\tv_mul_f32 %0, %3, %0\n\tv_accvgpr_write_b32 a54, %0\n\t" "v_accvgpr_read_b32 %1, a55\n\tv_mul_f32 %1, %3, %1\n\tv_accvgpr_write_b32 a55, %1\n\t" "v_accvgpr_read_b32 %0, a56\n\tv_mul_f32 %0, %3, %0\n\tv_accvgpr_write_b32 a56, %0\n\t" "v_accvgpr_read_b32 %1, a57\n\tv_mul_f32 %1, %3, %1\n\tv_accvgpr_write_b32 a57, %1\n\t" "v_accvgpr_read_b32 %0, a58\n\tv_mul_f32 %0, %3, %0\n\tv_accvgpr_write_b32 a58, %0\n\t" "v_accvgpr_read_b32 %1, a59\n\tv_mul_f32 %1, %3, %1\n\tv_accvgpr_write_b32 a59, %1\n\t" "v_accvgpr_read_b32 %0, a60\n\tv_mul_f32 %0, %3, %0\n\tv_accvgpr_write_b32 a60, %0\n\t" "v_accvgpr_read_b32 %1, a61\n\tv_mul_f32 %1, %3, %1\n\tv_accvgpr_write_b32 a61, %1\n\t" "v_accvgpr_read_b32 %0, a62\n\tv_mul_f32 %0, %3, %0\n\tv_accvgpr_write_b32 a62, %0\n\t" "v_accvgpr_read_b32 %1, a63\n\tv_mul_f32 %1, %3, %1\n\tv_accvgpr_write_b32 a63, %1\n\t" "v_accvgpr_read_b32 %0, a64\n\tv_mul_f32 %0, %2, %0\n\tv_accvgpr_write_b32 a64, %0\n\t" "v_accvgpr_read_b32 %1, a65\n\tv_mul_f32 %1, %2, %1\n\tv_accvgpr_write_b32 a65, %1\n\t" "v_accvgpr_read_b32 %0, a66\n\tv_mul_f32 %0, %2, %0\n\tv_accvgpr_write_b32 a66, %0\n\t" "v_accvgpr_read_b32 %1, a67\n\tv_mul_f32 %1, %2, %1\n\tv_accvgpr_write_b32 a67, %1\n\t" "v_accvgpr_read_b32 %0, a68\n\tv_mul_f32 %0, %2, %0\n\tv_accvgpr_write_b32 a68, %0\n\t" "v_accvgpr_read_b32 %1, a69\n\tv_mul_f32 %1, %2, %1\n\tv_accvgpr_write_b32 a69, %1\n\t" "v_accvgpr_read_b32 %0, a70\n\tv_mul_f32 %0, %2, %0\n\tv_accvgpr_write_b32 a70, %0\n\t" "v_accvgpr_read_b32 %1, a71\n\tv_mul_f32 %1, %2, %1\n\tv_accvgpr_write_b32 a71, %1\n\t" "v_accvgpr_read_b32 %0, a72\n\tv_mul_f32 %0, %2, %0\n\tv_accvgpr_write_b32 a72, %0\n\t" "v_accvgpr_read_b32 %1, a73\n\tv_mul_f32 %1, %2, %1\n\tv_accvgpr_write_b32 a73, %1\n\t" "v_accvgpr_read_b32 %0, a74\n\tv_mul_f32 %0, %2, %0\n\tv_accvgpr_write_b32 a74, %0\n\t" "v_accvgpr_read_b32 %1, a75\n\tv_mul_f32 %1, %2, %1\n\tv_accvgpr_write_b32 a75, %1\n\t" "v_accvgpr_read_b32 %0, a76\n\tv_mul_f32 %0, %2, %0\n\tv_accvgpr_write_b32 a76, %0\n\t" "v_accvgpr_read_b32 %1, a77\n\tv_mul_f32 %1, %2, %1\n\tv_accvgpr_write_b32 a77, %1\n\t" "v_accvgpr_read_b32 %0, a78\n\tv_mul_f32 %0, %2, %0\n\tv_accvgpr_write_b32 a78, %0\n\t" "v_accvgpr_read_b32 %1, a79\n\tv_mul_f32 %1, %2, %1\n\tv_accvgpr_write_b32 a79, %1\n\t" "v_accvgpr_read_b32 %0, a80\n\tv_mul_f32 %0, %3, %0\n\tv_accvgpr_write_b32 a80, %0\n\t" "v_accvgpr_read_b32 %1, a81\n\tv_mul_f32 %1, %3, %1\n\tv_accvgpr_write_b32 a81, %1\n\t" "v_accvgpr_read_b32 %0, a82\n\tv_mul_f32 %0, %3, %0\n\tv_accvgpr_write_b32 a82, %0\n\t" "v_accvgpr_read_b32 %1, a83\n\tv_mul_f32 %1, %3, %1\n\tv_accvgpr_write_b32 a83, %1\n\t" "v_accvgpr_read_b32 %0, a84\n\tv_mul_f32 %0, %3, %0\n\tv_accvgpr_write_b32 a84, %0\n\t" "v_accvgpr_read_b32 %1, a85\n\tv_mul_f32 %1, %3, %1\n\tv_accvgpr_write_b32 a85, %1\n\t" "v_accvgpr_read_b32 %0, a86\n\tv_mul_f32 %0, %3, %0\n\tv_accvgpr_write_b32 a86, %0\n\t" "v_accvgpr_read_b32 %1, a87\n\tv_mul_f32 %1, %3, %1\n\tv_accvgpr_write_b32 a87, %1\n\t" "v_accvgpr_read_b32 %0, a88\n\tv_mul_f32 %0, %3, %0\n\tv_accvgpr_write_b32 a88, %0\n\t" "v_accvgpr_read_b32 %1, a89\n\tv_mul_f32 %1, %3, %1\n\tv_accvgpr_write_b32 a89, %1\n\t" "v_accvgpr_read_b32 %0, a90\n\tv_mul_f32 %0, %3, %0\n\tv_accvgpr_write_b32 a90, %0\n\t" "v_accvgpr_read_b32 %1, a91\n\tv_mul_f32 %1, %3, %1\n\tv_accvgpr_write_b32 a91, %1\n\t" "v_accvgpr_read_b32 %0, a92\n\tv_mul_f32 %0, %3, %0\n\tv_accvgpr_write_b32 a92, %0\n\t" "v_accvgpr_read_b32 %1, a93\n\tv_mul_f32 %1, %3, %1\n\tv_accvgpr_write_b32 a93, %1\n\t" "v_accvgpr_read_b32 %0, a94\n\tv_mul_f32 %0, %3, %0\n\tv_accvgpr_write_b32 a94, %0\n\t" "v_accvgpr_read_b32 %1, a95\n\tv_mul_f32 %1, %3, %1\n\tv_accvgpr_write_b32 a95, %1\n\t" "v_accvgpr_read_b32 %0, a96\n\tv_mul_f32 %0, %2, %0\n\tv_accvgpr_write_b32 a96, %0\n\t" "v_accvgpr_read_b32 %1, a97\n\tv_mul_f32 %1, %2, %1\n\tv_accvgpr_write_b32 a97, %1\n\t" "v_accvgpr_read_b32 %0, a98\n\tv_mul_f32 %0, %2, %0\n\tv_accvgpr_write_b32 a98, %0\n\t" "v_accvgpr_read_b32 %1, a99\n\tv_mul_f32 %1, %2, %1\n\tv_accvgpr_write_b32 a99, %1\n\t" "v_accvgpr_read_b32 %0, a100\n\tv_mul_f32 %0, %2, %0\n\tv_accvgpr_write_b32 a100, %0\n\t" "v_accvgpr_read_b32 %1, a101\n\tv_mul_f32 %1, %2, %1\n\tv_accvgpr_write_b32 a101, %1\n\t" "v_accvgpr_read_b32 %0, a102\n\tv_mul_f32 %0, %2, %0\n\tv_accvgpr_write_b32 a102, %0\n\t" "v_accvgpr_read_b32 %1, a103\n\tv_mul_f32 %1, %2, %1\n\tv_accvgpr_write_b32 a103, %1\n\t" "v_accvgpr_read_b32 %0, a104\n\tv_mul_f32 %0, %2, %0\n\tv_accvgpr_write_b32 a104, %0\n\t" "v_accvgpr_read_b32 %1, a105\n\tv_mul_f32 %1, %2, %1\n\tv_accvgpr_write_b32 a105, %1\n\t" "v_accvgpr_read_b32 %0, a106\n\tv_mul_f32 %0, %2, %0\n\tv_accvgpr_write_b32 a106, %0\n\t" "v_accvgpr_read_b32 %1, a107\n\tv_mul_f32 %1, %2, %1\n\tv_accvgpr_write_b32 a107, %1\n\t" "v_accvgpr_read_b32 %0, a108\n\tv_mul_f32 %0, %2, %0\n\tv_accvgpr_write_b32 a108, %0\n\t" "v_accvgpr_read_b32 %1, a109\n\tv_mul_f32 %1, %2, %1\n\tv_accvgpr_write_b32 a109, %1\n\t" "v_accvgpr_read_b32 %0, a110\n\tv_mul_f32 %0, %2, %0\n\tv_accvgpr_write_b32 a110, %0\n\t" "v_accvgpr_read_b32 %1, a111\n\tv_mul_f32 %1, %2, %1\n\tv_accvgpr_write_b32 a111, %1\n\t" "v_accvgpr_read_b32 %0, a112\n\tv_mul_f32 %0, %3, %0\n\tv_accvgpr_write_b32 a112, %0\n\t" "v_accvgpr_read_b32 %1, a113\n\tv_mul_f32 %1, %3, %1\n\tv_accvgpr_write_b32 a113, %1\n\t" "v_accvgpr_read_b32 %0, a114\n\tv_mul_f32 %0, %3, %0\n\tv_accvgpr_write_b32 a114, %0\n\t" "v_accvgpr_read_b32 %1, a115\n\tv_mul_f32 %1, %3, %1\n\tv_accvgpr_write_b32 a115, %1\n\t" "v_accvgpr_read_b32 %0, a116\n\tv_mul_f32 %0, %3, %0\n\tv_accvgpr_write_b32 a116, %0\n\t" "v_accvgpr_read_b32 %1, a117\n\tv_mul_f32 %1, %3, %1\n\tv_accvgpr_write_b32 a117, %1\n\t" "v_accvgpr_read_b32 %0, a118\n\tv_mul_f32 %0, %3, %0\n\tv_accvgpr_write_b32 a118, %0\n\t" "v_accvgpr_read_b32 %1, a119\n\tv_mul_f32 %1, %3, %1\n\tv_accvgpr_write_b32 a119, %1\n\t" "v_accvgpr_read_b32 %0, a120\n\tv_mul_f32 %0, %3, %0\n\tv_accvgpr_write_b32 a120, %0\n\t" "v_accvgpr_read_b32 %1, a121\n\tv_mul_f32 %1, %3, %1\n\tv_accvgpr_write_b32 a121, %1\n\t" "v_accvgpr_read_b32 %0, a122\n\tv_mul_f32 %0, %3, %0\n\tv_accvgpr_write_b32 a122, %0\n\t" "v_accvgpr_read_b32 %1, a123\n\tv_mul_f32 %1, %3, %1\n\tv_accvgpr_write_b32 a123, %1\n\t" "v_accvgpr_read_b32 %0, a124\n\tv_mul_f32 %0, %3, %0\n\tv_accvgpr_write_b32 a124, %0\n\t" "v_accvgpr_read_b32 %1, a125\n\tv_mul_f32 %1, %3, %1\n\tv_accvgpr_write_b32 a125, %1\n\t" "v_accvgpr_read_b32 %0, a126\n\tv_mul_f32 %0, %3, %0\n\tv_accvgpr_write_b32 a126, %0\n\t" "v_accvgpr_read_b32 %1, a127\n\tv_mul_f32 %1, %3, %1\n\tv_accvgpr_write_b32 a127, %1\n\t"

#define STAMP(i)

DI unsigned rfl(unsigned x) { return __builtin_amdgcn_readfirstlane(x); }
DI unsigned cvtpk(float lo, float hi) {
  f32x2 v = {lo, hi}; bf16x2 b = __builtin_convertvector(v, bf16x2); return __builtin_bit_cast(unsigned, b);
}
DI float max3f(float a, float b, float c) { float r; asm("v_max3_f32 %0, %1, %2, %3" : "=v"(r) : "v"(a), "v"(b), "v"(c)); return r; }
DI float max2f(float a, float b) { float r; asm("v_max_f32_e32 %0, %1, %2" : "=v"(r) : "v"(a), "v"(b)); return r; }
DI float fadd_s(float a, float b) { float r; asm("v_add_f32_e32 %0, %1, %2" : "=v"(r) : "v"(a), "v"(b)); return r; }
DI float fmul_s(float a, float b) { float r; asm("v_mul_f32_e32 %0, %1, %2" : "=v"(r) : "v"(a), "v"(b)); return r; }
DI float fsub_s(float a, float b) { float r; asm("v_sub_f32_e32 %0, %1, %2" : "=v"(r) : "v"(a), "v"(b)); return r; }
DI float w4_aone() { unsigned l; asm volatile("v_mbcnt_lo_u32_b32 %0, -1, 0\n\tv_mbcnt_hi_u32_b32 %0, -1, %0" : "=v"(l)); return l < 32u ? 1.f : 0.f; }
DI void negm_bcast(f32x16& negm, float aone, float neg_mhat) {
  asm volatile("s_nop 1\n\tv_mfma_f32_32x32x2_f32 %0, %1, %2, 0" : "+v"(negm) : "v"(aone), "v"(neg_mhat));
}

struct W4C {
  unsigned vkb[4], vvb, vksrc[2], vvsrc, m0k, m0v, sofk, sofv, ldsb;
  __amdgpu_buffer_rsrc_t srdK, srdV;
  f32x16 S[2][2][2];
  f32x16 NEGM[2];
  bf16x8 Vf[4][4];
  bf16x8 Pk[2][4];
  bf16x4 vlo[4][4], vup[4][4];
  float mhat[2], ls[2][2], fct[2]; bool pend;
  float pmx0[2], pmx1[2], rmax[2];
  unsigned vvsrc2[2], sofv2, sofk3;
};

constexpr int AO_(int db, int qb) { return 16 * (2 * db + qb); }
constexpr int AQ_(int qb, int d0) { return 128 + 32 * qb + 4 * d0; }
constexpr int AK_(int kb, int d0) { return 192 + 32 * kb + 4 * d0; }

template <int I> DI void tr_read(W4C& c, unsigned vslot) {
  constexpr int ks = I >> 3, db = (I >> 1) & 3;
  if constexpr ((I & 1) == 0) asm volatile("ds_read_b64_tr_b16 %0, %1 offset:%2" : "=v"(c.vlo[db][ks]) : "v"(c.vvb + vslot), "i"(4096 * ks + 512 * db) : "memory");
  else                        asm volatile("ds_read_b64_tr_b16 %0, %1 offset:%2" : "=v"(c.vup[db][ks]) : "v"(c.vvb + vslot), "i"(4096 * ks + 2048 + 512 * db) : "memory");
}
template <int I> DI void k_read(W4C& c, unsigned slot) {
  constexpr int kb = I >> 3, d0 = I & 7;
  asm volatile("ds_read_b128 a[%c1:%c2], %0 offset:%c3" :: "v"(c.vkb[d0 & 3] + slot), "i"(AK_(kb, d0)), "i"(AK_(kb, d0) + 3), "i"(8192 * kb + 128 * (d0 >> 2)) : "memory", W4_ACLOB_HI);
}
template <int X, bool FIRST, int M> DI void qk_mfma(W4C& c) {
  constexpr int d0 = M >> 2, kb = (M >> 1) & 1, qb = M & 1, ak = AK_(kb, d0), aq = AQ_(qb, d0);
  if constexpr (d0 == 0) {
    if constexpr (FIRST) asm volatile("v_mfma_f32_32x32x16_bf16 %0, a[%c1:%c2], a[%c3:%c4], 0" : "=&v"(c.S[X][kb][qb]) : "i"(ak), "i"(ak + 3), "i"(aq), "i"(aq + 3) : W4_ACLOB, W4_ACLOB_HI);
    else                 asm volatile("v_mfma_f32_32x32x16_bf16 %0, a[%c2:%c3], a[%c4:%c5], %1" : "=&v"(c.S[X][kb][qb]) : "v"(c.NEGM[qb]), "i"(ak), "i"(ak + 3), "i"(aq), "i"(aq + 3) : W4_ACLOB, W4_ACLOB_HI);
  } else asm volatile("v_mfma_f32_32x32x16_bf16 %0, a[%c1:%c2], a[%c3:%c4], %0" : "+v"(c.S[X][kb][qb]) : "i"(ak), "i"(ak + 3), "i"(aq), "i"(aq + 3) : W4_ACLOB, W4_ACLOB_HI);
}
template <int M, bool ZERO> DI void pv_mfma(W4C& c) {
  constexpr int ks = M >> 3, db = (M >> 1) & 3, qb = M & 1, ao = AO_(db, qb);
  if constexpr (ZERO && ks == 0) asm volatile("v_mfma_f32_32x32x16_bf16 a[%c2:%c3], %0, %1, 0" :: "v"(c.Vf[db][ks]), "v"(c.Pk[qb][ks]), "i"(ao), "i"(ao + 15) : W4_ACLOB);
  else                         asm volatile("v_mfma_f32_32x32x16_bf16 a[%c2:%c3], %0, %1, a[%c2:%c3]" :: "v"(c.Vf[db][ks]), "v"(c.Pk[qb][ks]), "i"(ao), "i"(ao + 15) : W4_ACLOB);
}
constexpr int E_kb(int e) { return e >> 5; }
constexpr int E_q(int e)  { return (e >> 4) & 1; }
constexpr int E_r(int e)  { return e & 15; }
template <int X, int E> DI void exp1(W4C& c) { c.S[X][E_kb(E)][E_q(E)][E_r(E)] = __builtin_amdgcn_exp2f(c.S[X][E_kb(E)][E_q(E)][E_r(E)]); }
template <int X, int E> DI void add1(W4C& c) { c.ls[E_q(E)][E & 1] = fadd_s(c.ls[E_q(E)][E & 1], c.S[X][E_kb(E)][E_q(E)][E_r(E)]); }
template <int X, int P> DI void cvt1(W4C& c) {
  constexpr int e = 2 * P, kb = E_kb(e), q = E_q(e), r = E_r(e), ks = 2 * kb + (r >> 3), d = (r & 7) >> 1;
  u32x4 w = __builtin_bit_cast(u32x4, c.Pk[q][ks]);
  w[d] = cvtpk(c.S[X][kb][q][r], c.S[X][kb][q][r + 1]);
  c.Pk[q][ks] = __builtin_bit_cast(bf16x8, w);
  asm volatile("" :: "v"(w[d]));
}
template <bool FIRST> DI void max_group(float& m0, float& m1, const f32x16& s0, const f32x16& s1, const int r) {
  if constexpr (FIRST) asm("v_max3_f32 %0, %2, %3, %6\n\tv_max3_f32 %1, %4, %5, %7\n\tv_max3_f32 %0, %0, %8, %9" : "=&v"(m0), "=&v"(m1)
                           : "v"(s0[r]), "v"(s0[r + 1]), "v"(s0[r + 2]), "v"(s0[r + 3]), "v"(s1[r]), "v"(s1[r + 1]), "v"(s1[r + 2]), "v"(s1[r + 3]));
  else asm("v_max3_f32 %0, %0, %2, %3\n\tv_max3_f32 %1, %1, %4, %5\n\tv_max3_f32 %0, %0, %6, %7\n\tv_max3_f32 %1, %1, %8, %9" : "+v"(m0), "+v"(m1)
           : "v"(s0[r]), "v"(s0[r + 1]), "v"(s0[r + 2]), "v"(s0[r + 3]), "v"(s1[r]), "v"(s1[r + 1]), "v"(s1[r + 2]), "v"(s1[r + 3]));
}
#define SET_M0(V) asm volatile("s_mov_b32 m0, %0" :: "s"(V) : "m0", "memory")
#define DMA_OFF_NT(VOFFV, SRD, SOFF, OFF) asm volatile("buffer_load_dwordx4 %0, %1, %2 offen offset:%3 nt lds" :: "v"(VOFFV), "s"(SRD), "s"(SOFF), "i"(OFF) : "memory")

template <int X, bool DMA, int M> DI void gapA(W4C& c, unsigned vslot, unsigned dm0, unsigned dso) {
  constexpr int Xp = X ^ 1;
  if constexpr (DMA && M >= 24 && (M & 1) == 0) SET_M0(dm0);
  qk_mfma<X, false, M>(c);
  GAP();
  if constexpr (DMA && M >= 24 && (M & 1) == 0) { constexpr int q = (M - 24) >> 1; DMA_OFF_NT(c.vvsrc2[q & 1], c.srdV, dso, 1024 * q); }
  if constexpr (M == 8) asm volatile("" :: "v"(c.NEGM[0]), "v"(c.NEGM[1]));
  if constexpr (M < 12) { tr_read<M>(c, vslot); exp1<Xp, 40 + 2 * M>(c); cvt1<Xp, M>(c); exp1<Xp, 41 + 2 * M>(c); }
  else if constexpr (M < 22) { constexpr int a0 = 18 + 3 * (M - 12);
    tr_read<12 + 2 * (M - 12)>(c, vslot); add1<Xp, a0>(c); add1<Xp, a0 + 1>(c); cvt1<Xp, M>(c); tr_read<13 + 2 * (M - 12)>(c, vslot); add1<Xp, a0 + 2>(c); }
  else { cvt1<Xp, M>(c); if constexpr (M < 30) { add1<Xp, 48 + 2 * (M - 22)>(c); add1<Xp, 49 + 2 * (M - 22)>(c); } }
  GAP();
}
#define W4_VLO "+v"(c.vlo[0][0]), "+v"(c.vlo[0][1]), "+v"(c.vlo[0][2]), "+v"(c.vlo[0][3]), "+v"(c.vlo[1][0]), "+v"(c.vlo[1][1]), "+v"(c.vlo[1][2]), "+v"(c.vlo[1][3]), \
               "+v"(c.vlo[2][0]), "+v"(c.vlo[2][1]), "+v"(c.vlo[2][2]), "+v"(c.vlo[2][3]), "+v"(c.vlo[3][0]), "+v"(c.vlo[3][1]), "+v"(c.vlo[3][2]), "+v"(c.vlo[3][3])
#define W4_VUP "+v"(c.vup[0][0]), "+v"(c.vup[0][1]), "+v"(c.vup[0][2]), "+v"(c.vup[0][3]), "+v"(c.vup[1][0]), "+v"(c.vup[1][1]), "+v"(c.vup[1][2]), "+v"(c.vup[1][3]), \
               "+v"(c.vup[2][0]), "+v"(c.vup[2][1]), "+v"(c.vup[2][2]), "+v"(c.vup[2][3]), "+v"(c.vup[3][0]), "+v"(c.vup[3][1]), "+v"(c.vup[3][2]), "+v"(c.vup[3][3])
template <int X, bool DMA, int VM, int... Ms> DI void phaseA_(W4C& c, unsigned sl_m1, unsigned sl_p2, unsigned tb, iseq<Ms...>) {
  const unsigned dm0 = c.m0v + sl_p2, dso = tb + c.sofv2;
  (gapA<X, DMA, Ms>(c, sl_m1, dm0, dso), ...);
  asm volatile("s_waitcnt vmcnt(%16) lgkmcnt(0)" : W4_VLO : "i"(VM) : "memory"); asm volatile("s_barrier" : W4_VUP :: "memory");
  GAP();
  #pragma unroll
  for (int ks = 0; ks < 4; ++ks)
    #pragma unroll
    for (int db = 0; db < 4; ++db) c.Vf[db][ks] = __builtin_shufflevector(c.vlo[db][ks], c.vup[db][ks], 0, 1, 2, 3, 4, 5, 6, 7);
}
template <int X, bool DMA, int VM> DI void phaseA(W4C& c, unsigned sl_m1, unsigned sl_p2, unsigned tb) { phaseA_<X, DMA, VM>(c, sl_m1, sl_p2, tb, make_iseq<32>{}); }

template <int X, bool PEEL, bool ZERO1, bool DMA, bool KR, int M> DI void gapB(W4C& c, const unsigned kslot_rd, unsigned dm0, unsigned dso) {
  if constexpr (DMA && (M == 0 || M == 12 || M == 13)) SET_M0(dm0);
  if constexpr (!PEEL) pv_mfma<M, ZERO1>(c); else asm volatile("s_nop 0");
  GAP();
  if constexpr (DMA && M == 0) { DMA_OFF_NT(c.vksrc[0], c.srdK, dso, 0); DMA_OFF_NT(c.vksrc[1], c.srdK, dso, 1024); }
  if constexpr (DMA && M == 12) DMA_OFF_NT(c.vksrc[0], c.srdK, dso, 2048);
  if constexpr (DMA && M == 13) DMA_OFF_NT(c.vksrc[1], c.srdK, dso, 3072);
  if constexpr (M >= 1 && M <= 8) {
    constexpr int i = M - 1, q = i >> 2, r = 4 * (i & 3);
    if constexpr (KR) k_read<2 * i>(c, kslot_rd);
    max_group<(i & 3) == 0>(c.pmx0[q], c.pmx1[q], c.S[X][0][q], c.S[X][1][q], r);
    if constexpr (KR) k_read<2 * i + 1>(c, kslot_rd);
  }
  if constexpr (M == 9 || M == 10) {
    constexpr int q = M - 9; const float mm = max2f(c.pmx0[q], c.pmx1[q]);
    auto pr = __builtin_amdgcn_permlane32_swap(__float_as_uint(mm), __float_as_uint(mm), false, false);
    c.rmax[q] = max2f(__uint_as_float(pr[0]), __uint_as_float(pr[1]));
  }
  if constexpr (M == 11) {
    if constexpr (PEEL) {
      #pragma unroll
      for (int q = 0; q < 2; ++q) { c.mhat[q] = c.rmax[q];
        #pragma unroll
        for (int kb = 0; kb < 2; ++kb) { _Pragma("unroll") for (int e = 0; e < 16; ++e) c.S[X][kb][q][e] = fsub_s(c.S[X][kb][q][e], c.rmax[q]); }
        negm_bcast(c.NEGM[q], w4_aone(), -c.mhat[q]);
      }
    } else {
      const bool grew = max2f(c.rmax[0], c.rmax[1]) > THR;
      if (__builtin_expect(__any((int)grew), 0)) {
        #pragma unroll
        for (int q = 0; q < 2; ++q) { const float dl = max2f(c.rmax[q], 0.f); c.mhat[q] = fadd_s(c.mhat[q], dl); c.fct[q] = __builtin_amdgcn_exp2f(-dl);
          #pragma unroll
          for (int kb = 0; kb < 2; ++kb) { _Pragma("unroll") for (int e = 0; e < 16; ++e) c.S[X][kb][q][e] = fsub_s(c.S[X][kb][q][e], dl); }
          negm_bcast(c.NEGM[q], w4_aone(), -c.mhat[q]);
          asm volatile("s_nop 0" : "+v"(c.fct[q]));
          c.ls[q][0] = fmul_s(c.ls[q][0], c.fct[q]); c.ls[q][1] = fmul_s(c.ls[q][1], c.fct[q]);
        }
        c.pend = true;
      }
    }
  }
  if constexpr (M >= 12) {
    exp1<X, 2 * (M - 12)>(c);
    if constexpr (M >= 14) add1<X, M - 14>(c);
    exp1<X, 2 * (M - 12) + 1>(c);
  }
  if constexpr (M == 31) asm volatile("" :: "v"(c.S[X][0][0]), "v"(c.S[X][0][1]), "v"(c.S[X][1][0]), "v"(c.NEGM[0]), "v"(c.NEGM[1]), "v"(c.ls[0][0]), "v"(c.ls[0][1]), "v"(c.ls[1][0]), "v"(c.ls[1][1]));
  GAP();
}
template <int X, bool PEEL, bool ZERO1, bool DMA, bool KR, int... Ms> DI void phaseB_(W4C& c, unsigned sl_p1, unsigned sl_p3, unsigned tb, iseq<Ms...>) {
  const unsigned dm0 = c.m0k + sl_p3, dso = tb + c.sofk3;
  if constexpr (!PEEL) asm volatile("s_nop 1" ::: "memory");
  GAP();
  (gapB<X, PEEL, ZERO1, DMA, KR, Ms>(c, sl_p1, dm0, dso), ...);
  if constexpr (!PEEL) {
    if (__builtin_expect(c.pend, 0)) {
      float t0, t1;
      asm volatile("s_nop 7\n\ts_nop 7\n\t" W4_RESCALE_TEXT "s_nop 1" : "=&v"(t0), "=&v"(t1) : "v"(c.fct[0]), "v"(c.fct[1]) : W4_ACLOB);
      c.pend = false;
    }
  }
  asm volatile("s_waitcnt lgkmcnt(0)" ::: "memory");
  GAP();
}
template <int X, bool PEEL, bool ZERO1, bool DMA, bool KR> DI void phaseB(W4C& c, unsigned sl_p1, unsigned sl_p3, unsigned tb) { phaseB_<X, PEEL, ZERO1, DMA, KR>(c, sl_p1, sl_p3, tb, make_iseq<32>{}); }

template <int... Is> DI void tr_reads(W4C& c, unsigned vslot, iseq<Is...>) { (tr_read<Is>(c, vslot), ...); }
template <int X, int... Es> DI void exps_from40(W4C& c, iseq<Es...>) { (exp1<X, 40 + Es>(c), ...); }
template <int X, int... Ps> DI void cvts(W4C& c, iseq<Ps...>) { (cvt1<X, Ps>(c), ...); }
template <int X, int... Es> DI void adds_from18(W4C& c, iseq<Es...>) { (add1<X, 18 + Es>(c), ...); }

struct QC {
  unsigned vxb[4], vwb, vxsrc[2], vwsrc, m0x, m0w, sofx, sofw;
  __amdgpu_buffer_rsrc_t srdX, srdW, srdO;
  unsigned vwo, sofo, rot, wo[32];
  bf16x4 alo[2][4], aup[2][4];
  bf16x8 xb[2][2];
  f32x16 acc[4][2];
};
#define DMA_ASM(M0V, VOFFV, SRD, SOFF) asm volatile("s_mov_b32 m0, %0\n\ts_nop 0\n\tbuffer_load_dwordx4 %1, %2, %3 offen lds" :: "s"(M0V), "v"(VOFFV), "s"(SRD), "s"(SOFF) : "m0", "memory")
#define DMA_ASM_NT(M0V, VOFFV, SRD, SOFF) asm volatile("s_mov_b32 m0, %0\n\ts_nop 0\n\tbuffer_load_dwordx4 %1, %2, %3 offen nt lds" :: "s"(M0V), "v"(VOFFV), "s"(SRD), "s"(SOFF) : "m0", "memory")
template <int CI, int P> DI void q_dma(QC& q) {
  if constexpr (P < 8) DMA_ASM(q.m0x + QBUF(CI) + 1024u * P, q.vxsrc[P & 1], q.srdX, q.sofx + 8192u * P + 128u * ((CI + q.rot) & 7u));
  else { constexpr int pp = P - 8; DMA_ASM(q.m0w + QBUF(CI) + 1024u * pp, q.vwsrc, q.srdW, q.sofw + 524288u * ((CI + q.rot) & 7u) + 65536u * (pp >> 1) + 128u * (pp & 1)); }
}
template <int T, int P> DI void kv_dma(W4C& c) {
  if constexpr (P < 4) DMA_ASM_NT(c.m0k + tslot(T) + 1024u * P, c.vksrc[P & 1], c.srdK, c.sofk + 16384u * T + 1024u * P);
  else { constexpr int pp = P - 4; DMA_ASM_NT(c.m0v + tslot(T) + 1024u * pp, c.vvsrc, c.srdV, c.sofv + 16384u * T + 2048u * (pp >> 1) + 128u * (pp & 1)); }
}
template <int CI, int KS, int R> DI void q_read(QC& q) {
  constexpr int PAR = KS & 1;
  if constexpr (R < 8) {
    constexpr int db = R >> 1;
    if constexpr ((R & 1) == 0) asm volatile("ds_read_b64_tr_b16 %0, %1 offset:%2" : "=v"(q.alo[PAR][db]) : "v"(q.vwb + QBUF(CI)), "i"(4096 * KS + 512 * db) : "memory");
    else                        asm volatile("ds_read_b64_tr_b16 %0, %1 offset:%2" : "=v"(q.aup[PAR][db]) : "v"(q.vwb + QBUF(CI)), "i"(4096 * KS + 512 * db + 256) : "memory");
  } else {
    constexpr int qb = R - 8;
    asm volatile("ds_read_b128 %0, %1 offset:%2" : "=v"(q.xb[PAR][qb]) : "v"(q.vxb[KS] + QBUF(CI)), "i"(4096 * qb) : "memory");
  }
}
template <int CI, int KS, int M> DI void q_mfma(QC& q) {
  constexpr int PAR = KS & 1, db = M >> 1, qb = M & 1;
  const bf16x8 a = __builtin_shufflevector(q.alo[PAR][db], q.aup[PAR][db], 0, 1, 2, 3, 4, 5, 6, 7);
  if constexpr (CI == 0 && KS == 0) asm volatile("v_mfma_f32_32x32x16_bf16 %0, %1, %2, 0" : "=&v"(q.acc[db][qb]) : "v"(a), "v"(q.xb[PAR][qb]));
  else                              asm volatile("v_mfma_f32_32x32x16_bf16 %0, %1, %2, %0" : "+v"(q.acc[db][qb]) : "v"(a), "v"(q.xb[PAR][qb]));
}
#define ROTEXPR (0u)
constexpr int sched_dma(int ci, int ks, int m) {
  const int j = m == 1 ? 0 : (m == 4 ? 1 : (m == 7 ? 2 : -1));
  if (ci == 7 && ks == 3) return (m & 1) ? 1000 + 16 + (m >> 1) : -1;
  if (ci == 0 && ks < 3) return (m & 1) ? 200 + 4 * ks + (m >> 1) : (m == 2 ? 1000 + ks : ((m == 6 && ks == 2) ? 1003 : -1));
  if (j < 0) return -1;
  const int i = ks == 3 ? ci : ci - 1, st = ks == 3 ? 0 : ks + 1, p = 3 * st + j;
  if (i < 0) return -1;
  if (i + 3 < 8) return 100 * (i + 3) + p;
  if (i == 5) return p < 4 ? 1000 + 4 + p : -1;
  if (i == 6) return p < 8 ? 1000 + 8 + p : -1;
  return -1;
}
constexpr int sched_wo(int ci, int ks, int m) { return (ci >= 1 && ci <= 4 && (m == 2 || m == 5)) ? ((ci - 1) * 4 + ks) * 2 + (m == 5) : -1; }
constexpr int sched_vm(int ci) {
  int n = 0;
  for (int c = 0; c < 2; ++c) for (int p = 0; p < 12; ++p) { n = (c == ci + 1) ? 0 : n + 1; }
  for (int c = 0; c <= ci; ++c) for (int ks = 0; ks < 4; ++ks) { if (c == ci && ks == 3) break;
    for (int m = 0; m < 8; ++m) {
      const int d = sched_dma(c, ks, m);
      if (d >= 0) n = ((d < 1000 && d / 100 == ci + 1) || (ci == 7 && d >= 1000 && d < 1004)) ? 0 : n + 1;
      if (sched_wo(c, ks, m) >= 0) n = (ci == 7) ? 0 : n + 1;
    } }
  return n;
}
template <int I> DI void wo_load(QC& q) {
  constexpr int i = I >> 3, j = I & 7;
  asm volatile("buffer_load_ushort %0, %1, %2, %3 offen offset:%4" : "=v"(q.wo[I]) : "v"(q.vwo), "s"(q.srdO), "s"(q.sofo + 8192u * (j >> 2)), "i"(1024 * (j & 3) + 64 * i) : "memory");
}
template <int CI, int KS, int M> DI void q_gap(QC& q, W4C& c) {
  q_mfma<CI, KS, M>(q);
  constexpr int NCI = KS == 3 ? CI + 1 : CI, NKS = (KS + 1) & 3;
  if constexpr (NCI < 8 && M < 5) { q_read<NCI, NKS, 2 * M>(q); q_read<NCI, NKS, 2 * M + 1>(q); }
  constexpr int D = sched_dma(CI, KS, M), W = sched_wo(CI, KS, M);
  if constexpr (D >= 1000) kv_dma<((D - 1000) >> 3), ((D - 1000) & 7)>(c);
  else if constexpr (D >= 0) q_dma<(D / 100), (D % 100)>(q);
  if constexpr (W >= 0) wo_load<W>(q);
  if constexpr (KS == 3 && CI == 7) { k_read<2 * M>(c, tslot(0)); k_read<2 * M + 1>(c, tslot(0)); }
  GAP();
}
template <int CI, int KS, int... Ms> DI void q_step_(QC& q, W4C& c, iseq<Ms...>) {
  constexpr int PAR = KS & 1;
  if constexpr (KS == 3) {
    constexpr int VM = sched_vm(CI);
    asm volatile("s_waitcnt vmcnt(%10) lgkmcnt(0)\n\ts_barrier" : "+v"(q.alo[PAR][0]), "+v"(q.alo[PAR][1]), "+v"(q.alo[PAR][2]), "+v"(q.alo[PAR][3]),
                 "+v"(q.aup[PAR][0]), "+v"(q.aup[PAR][1]), "+v"(q.aup[PAR][2]), "+v"(q.aup[PAR][3]), "+v"(q.xb[PAR][0]), "+v"(q.xb[PAR][1]) : "i"(VM) : "memory");
  } else {
    asm volatile("s_waitcnt lgkmcnt(0)" : "+v"(q.alo[PAR][0]), "+v"(q.alo[PAR][1]), "+v"(q.alo[PAR][2]), "+v"(q.alo[PAR][3]),
                 "+v"(q.aup[PAR][0]), "+v"(q.aup[PAR][1]), "+v"(q.aup[PAR][2]), "+v"(q.aup[PAR][3]), "+v"(q.xb[PAR][0]), "+v"(q.xb[PAR][1]) :: "memory");
  }
  GAP();
  (q_gap<CI, KS, Ms>(q, c), ...);
}
template <int CI> DI void q_chunk(QC& q, W4C& c) {
  q_step_<CI, 0>(q, c, make_iseq<8>{}); q_step_<CI, 1>(q, c, make_iseq<8>{}); q_step_<CI, 2>(q, c, make_iseq<8>{}); q_step_<CI, 3>(q, c, make_iseq<8>{});
}
template <int... Cs> DI void q_chunks(QC& q, W4C& c, iseq<Cs...>) { (q_chunk<Cs>(q, c), ...); }
template <int CI, int... Ps> DI void q_dmas(QC& q, iseq<Ps...>) { (q_dma<CI, Ps>(q), ...); }
template <int... Rs> DI void q_reads0(QC& q, iseq<Rs...>) { (q_read<0, 0, Rs>(q), ...); }

template <int BASE> DI void acc_read8(float (&o)[8]) {
  asm volatile("v_accvgpr_read_b32 %0, a[%c8]\n\tv_accvgpr_read_b32 %1, a[%c9]\n\tv_accvgpr_read_b32 %2, a[%c10]\n\tv_accvgpr_read_b32 %3, a[%c11]\n\t"
               "v_accvgpr_read_b32 %4, a[%c12]\n\tv_accvgpr_read_b32 %5, a[%c13]\n\tv_accvgpr_read_b32 %6, a[%c14]\n\tv_accvgpr_read_b32 %7, a[%c15]"
               : "=v"(o[0]), "=v"(o[1]), "=v"(o[2]), "=v"(o[3]), "=v"(o[4]), "=v"(o[5]), "=v"(o[6]), "=v"(o[7])
               : "i"(BASE), "i"(BASE + 1), "i"(BASE + 2), "i"(BASE + 3), "i"(BASE + 4), "i"(BASE + 5), "i"(BASE + 6), "i"(BASE + 7));
}
template <int AQ> DI void acc_write4(const u32x4& w) {
  asm volatile("v_accvgpr_write_b32 a[%c4], %0\n\tv_accvgpr_write_b32 a[%c5], %1\n\tv_accvgpr_write_b32 a[%c6], %2\n\tv_accvgpr_write_b32 a[%c7], %3"
               :: "v"(w[0]), "v"(w[1]), "v"(w[2]), "v"(w[3]), "i"(AQ), "i"(AQ + 1), "i"(AQ + 2), "i"(AQ + 3) : W4_ACLOB_HI);
}
template <int I> DI void q_convert(QC& q, float C) {
  constexpr int qb = I & 1, d0 = I >> 1, db = d0 >> 1, s = d0 & 1;
  u32x4 w;
  #pragma unroll
  for (int e = 0; e < 4; ++e) w[e] = cvtpk(fmul_s(q.acc[db][qb][8 * s + 2 * e], C), fmul_s(q.acc[db][qb][8 * s + 2 * e + 1], C));
  acc_write4<AQ_(qb, d0)>(w);
}
template <int M> DI void gapA0(W4C& c, QC& q, float C) {
  qk_mfma<0, true, M>(c);
  GAP();
  if constexpr (M < 28 && (M & 3) < 2) { q_convert<2 * ((M >> 2) + 1) + (M & 3)>(q, C); asm volatile("s_nop 1" ::: "memory"); }
  if constexpr (M >= 24 && (M & 1) == 0) kv_dma<2, 4 + ((M - 24) >> 1)>(c);
  GAP();
}
template <int... Ms> DI void phaseA0(W4C& c, QC& q, float C, iseq<Ms...>) {
  (gapA0<Ms>(c, q, C), ...);
  asm volatile("s_waitcnt vmcnt(16) lgkmcnt(0)\n\ts_barrier" ::: "memory");
  GAP();
}
template <int I> DI void o_store(const float (&il)[2], char* obase, unsigned voff) {
  constexpr int qb = I >> 3, kk = I & 7, db = kk >> 1, s = kk & 1;
  float o[8]; acc_read8<AO_(db, qb) + 8 * s>(o);
  u32x4 w;
  #pragma unroll
  for (int e = 0; e < 4; ++e) w[e] = cvtpk(fmul_s(o[2 * e], il[qb]), fmul_s(o[2 * e + 1], il[qb]));
  __builtin_amdgcn_raw_buffer_store_b128(w, __builtin_amdgcn_make_buffer_rsrc((void*)obase, (short)0, 512 * 1024, SRD_WORD3), voff + kk * 1024, qb * (256 * 1024), ST_AUX);
}
template <int J, int KS> DI void pv_last(W4C& c) {
  constexpr int db = J >> 1, qb = J & 1, ao = AO_(db, qb);
  asm volatile("v_mfma_f32_32x32x16_bf16 a[%c2:%c3], %0, %1, a[%c2:%c3]" :: "v"(c.Vf[db][KS]), "v"(c.Pk[qb][KS]), "i"(ao), "i"(ao + 15) : W4_ACLOB);
}
template <int J> DI void drain_acc(W4C& c, const float (&il)[2], char* obase, unsigned voff) {
  constexpr int Jp = J - 1;
  pv_last<J, 0>(c); GAP();
  pv_last<J, 1>(c); GAP();
  pv_last<J, 2>(c); GAP();
  if constexpr (J > 0) { asm volatile("s_nop 7\n\ts_nop 1" ::: "memory"); o_store<8 * (Jp & 1) + 2 * (Jp >> 1)>(il, obase, voff); }
  GAP();
  pv_last<J, 3>(c); GAP();
  if constexpr (J > 0) o_store<8 * (Jp & 1) + 2 * (Jp >> 1) + 1>(il, obase, voff);
  GAP();
}
template <int... Js> DI void drain_accs(W4C& c, const float (&il)[2], char* obase, unsigned voff, iseq<Js...>) { (drain_acc<Js>(c, il, obase, voff), ...); }

constexpr size_t WS_OF = 0, WS_WOF = (size_t)16 << 20;

extern "C" __global__ __launch_bounds__(256, 1)
void attn_fused(const unsigned short* __restrict__ X, const unsigned short* __restrict__ Kc, const unsigned short* __restrict__ Vc,
                const unsigned short* __restrict__ Wq, const unsigned short* __restrict__ Wo, char* __restrict__ ws) {
  extern __shared__ __attribute__((aligned(16))) char lds[];
  const unsigned tid = threadIdx.x, lane = tid & 63u, wid = tid >> 6, l32 = lane & 31u, hi = lane >> 5;
  const unsigned g = blockIdx.x, hd = 4u * (g & 7u) + ((g >> 3) & 3u), b = g >> 5;
  const float C = 0.12751743082f;
  STAMP(0);
  W4C c; QC q;
  c.ldsb = (unsigned)(size_t)lds;
  #pragma unroll
  for (int j = 0; j < 4; ++j) c.vkb[j] = c.ldsb + KOFF + 256u * l32 + 16u * ((2u * j + hi) ^ (l32 & 7u));
  c.vvb = c.ldsb + (((lane & 3u) << 3) | (((lane >> 2) & 3u) << 6) | (((lane >> 4) & 1u) << 5) | (hi << 8));
  #pragma unroll
  for (int par = 0; par < 2; ++par) c.vksrc[par] = 256u * (lane >> 4) + 16u * ((lane & 15u) ^ (4u * par + (lane >> 4)));
  c.vvsrc = 256u * ((lane >> 2) & 7u) + 64u * hi + 16u * (lane & 3u);
  c.m0k = rfl(c.ldsb + KOFF + 4096u * wid); c.m0v = rfl(c.ldsb + 4096u * wid);
  c.sofk = rfl(4096u * wid); c.sofv = rfl(4096u * wid);
  c.vvsrc2[0] = c.vvsrc + 896u; c.vvsrc2[1] = c.vvsrc; c.sofv2 = c.sofv + 32768u - 896u; c.sofk3 = c.sofk + 49152u;
  const size_t kvoff = ((size_t)b * NH + hd) * (SK * DH * 2);
  c.srdK = __builtin_amdgcn_make_buffer_rsrc((void*)((const char*)Kc + kvoff), (short)0, SK * DH * 2, SRD_WORD3);
  c.srdV = __builtin_amdgcn_make_buffer_rsrc((void*)((const char*)Vc + kvoff), (short)0, SK * DH * 2, SRD_WORD3);
  #pragma unroll
  for (int ks = 0; ks < 4; ++ks) q.vxb[ks] = c.ldsb + 8192u * wid + 128u * l32 + 16u * ((2u * ks + hi) ^ ((l32 >> 1) & 7u));
  { const unsigned p = lane & 3u, sp = ((p & 1u) << 1) | (p >> 1);
    q.vwb = c.ldsb + QWOFF + ((hi << 11) | (((lane >> 2) & 3u) << 6) | (((lane >> 4) & 1u) << 5) | (sp << 3)); }
  #pragma unroll
  for (int par = 0; par < 2; ++par) q.vxsrc[par] = 1024u * (lane >> 3) + 16u * ((lane & 7u) ^ (4u * par + (lane >> 4)));
  q.vwsrc = 8192u * ((lane >> 2) & 7u) + 64u * hi + 16u * (lane & 3u);
  q.m0x = rfl(c.ldsb + 8192u * wid); q.m0w = rfl(c.ldsb + QWOFF + 4096u * wid);
  q.sofx = rfl(65536u * wid); q.sofw = rfl(131072u * wid);
  q.srdX = __builtin_amdgcn_make_buffer_rsrc((void*)((const char*)X + (size_t)b * (SQ * DM * 2)), (short)0, SQ * DM * 2, SRD_WORD3);
  q.srdW = __builtin_amdgcn_make_buffer_rsrc((void*)((const char*)Wq + hd * 256u), (short)0, DM * NH * DH * 2 - (int)hd * 256, SRD_WORD3);
  q.srdO = __builtin_amdgcn_make_buffer_rsrc((void*)Wo, (short)0, NH * DH * DM * 2, SRD_WORD3);
  q.rot = ROTEXPR;
  q.vwo = 4096u * hi + 2u * l32; q.sofo = rfl(16384u * g + 256u * wid);
  asm volatile("s_nop 4" ::: "memory");
  q_dmas<0>(q, make_iseq<12>{});
  q_dmas<1>(q, make_iseq<12>{});
  STAMP(1);
  asm volatile("s_waitcnt vmcnt(12)\n\ts_barrier" ::: "memory");
  STAMP(2);
  q_reads0(q, make_iseq<10>{});
  q_chunks(q, c, make_iseq<8>{});
  STAMP(3);
  asm volatile("" : "+v"(q.wo[0]), "+v"(q.wo[1]), "+v"(q.wo[2]), "+v"(q.wo[3]), "+v"(q.wo[4]), "+v"(q.wo[5]), "+v"(q.wo[6]), "+v"(q.wo[7]),
                    "+v"(q.wo[8]), "+v"(q.wo[9]), "+v"(q.wo[10]), "+v"(q.wo[11]), "+v"(q.wo[12]), "+v"(q.wo[13]), "+v"(q.wo[14]), "+v"(q.wo[15]));
  asm volatile("" : "+v"(q.wo[16]), "+v"(q.wo[17]), "+v"(q.wo[18]), "+v"(q.wo[19]), "+v"(q.wo[20]), "+v"(q.wo[21]), "+v"(q.wo[22]), "+v"(q.wo[23]),
                    "+v"(q.wo[24]), "+v"(q.wo[25]), "+v"(q.wo[26]), "+v"(q.wo[27]), "+v"(q.wo[28]), "+v"(q.wo[29]), "+v"(q.wo[30]), "+v"(q.wo[31]));
  #pragma unroll
  for (int i = 0; i < 4; ++i) {
    u32x4 w;
    #pragma unroll
    for (int e = 0; e < 4; ++e) w[e] = q.wo[8 * i + 2 * e] | (q.wo[8 * i + 2 * e + 1] << 16);
    __builtin_amdgcn_raw_buffer_store_b128(w, __builtin_amdgcn_make_buffer_rsrc((void*)(ws + WS_WOF), (short)0, 4 << 20, SRD_WORD3), lane * 16u, rfl(((4u * wid + i) * 256u + g) * 1024u), ST_AUX);
  }
  asm volatile("s_nop 7\n\ts_nop 7" : "+v"(q.acc[0][0]), "+v"(q.acc[0][1]), "+v"(q.acc[1][0]), "+v"(q.acc[1][1]), "+v"(q.acc[2][0]), "+v"(q.acc[2][1]), "+v"(q.acc[3][0]), "+v"(q.acc[3][1]) :: "memory");
  q_convert<0>(q, C); q_convert<1>(q, C);
  asm volatile("s_waitcnt lgkmcnt(0)\n\ts_nop 1" ::: "memory");

  STAMP(4);
  c.mhat[0] = c.mhat[1] = 0.f; c.ls[0][0] = c.ls[0][1] = c.ls[1][0] = c.ls[1][1] = 0.f; c.fct[0] = c.fct[1] = 1.f; c.pend = false;
  phaseA0(c, q, C, make_iseq<32>{});                                   phaseB<0, true,  false, true, true>(c, tslot(1), tslot(3), 0u);
  phaseA<1, true, 16>(c, tslot(0), tslot(3), 16384u);                  phaseB<1, false, true,  true, true>(c, tslot(2), tslot(4), 16384u);
  STAMP(5);
  {
    unsigned s0 = tslot(1), s1 = tslot(2), s2 = tslot(3), s3 = tslot(4), s4 = tslot(5), tb = 32768u;
    for (int t = 2; t < 12; t += 2) {
      phaseA<0, true, 12>(c, s0, s3, tb);          phaseB<0, false, false, true, true>(c, s2, s4, tb);
      phaseA<1, true, 12>(c, s1, s4, tb + 16384u); phaseB<1, false, false, true, true>(c, s3, s0, tb + 16384u);
      const unsigned n0 = s2, n1 = s3, n2 = s4, n3 = s0, n4 = s1; s0 = n0; s1 = n1; s2 = n2; s3 = n3; s4 = n4; tb += 32768u;
    }
  }
  STAMP(6);
  phaseA<0, true, 12>(c, tslot(11), tslot(14), 12u * 16384u);  phaseB<0, false, false, true,  true>(c, tslot(13), tslot(15), 12u * 16384u);
  phaseA<1, true, 12>(c, tslot(12), tslot(15), 13u * 16384u);  phaseB<1, false, false, false, true>(c, tslot(14), 0u, 0u);
  phaseA<0, false, 4>(c, tslot(13), 0u, 0u);                   phaseB<0, false, false, false, true>(c, tslot(15), 0u, 0u);
  phaseA<1, false, 0>(c, tslot(14), 0u, 0u);                   phaseB<1, false, false, false, false>(c, 0u, 0u, 0u);
  STAMP(7);
  {
    const unsigned vslot = tslot(NT - 1);
    tr_reads(c, vslot, make_iseq<32>{});
    exps_from40<1>(c, make_iseq<24>{});
    cvts<1>(c, make_iseq<32>{});
    adds_from18<1>(c, make_iseq<46>{});
    float lsum[2];
    #pragma unroll
    for (int qb = 0; qb < 2; ++qb) { const float rs = fadd_s(c.ls[qb][0], c.ls[qb][1]);
      auto pr = __builtin_amdgcn_permlane32_swap(__float_as_uint(rs), __float_as_uint(rs), false, false);
      lsum[qb] = fadd_s(__uint_as_float(pr[0]), __uint_as_float(pr[1])); }
    asm volatile("s_waitcnt lgkmcnt(0)" : W4_VLO :: "memory"); asm volatile("" : W4_VUP :: "memory");
    GAP();
    #pragma unroll
    for (int ks = 0; ks < 4; ++ks)
      #pragma unroll
      for (int db = 0; db < 4; ++db) c.Vf[db][ks] = __builtin_shufflevector(c.vlo[db][ks], c.vup[db][ks], 0, 1, 2, 3, 4, 5, 6, 7);
    asm volatile("s_nop 1" ::: "memory");
    float il[2];
    il[0] = __builtin_amdgcn_rcpf(lsum[0]); il[1] = __builtin_amdgcn_rcpf(lsum[1]);
    asm volatile("s_nop 1" : "+v"(il[0]), "+v"(il[1]));
    char* obase = ws + WS_OF + (size_t)rfl(((b * 8u + 2u * wid) * 256u + hd * 8u) * 1024u);
    drain_accs(c, il, obase, lane * 16u, make_iseq<8>{});
    STAMP(8);
    asm volatile("s_nop 7\n\ts_nop 7" ::: "memory");
    o_store<8 + 6>(il, obase, lane * 16u); o_store<8 + 7>(il, obase, lane * 16u);
  }
}

extern "C" __global__ __launch_bounds__(512, 2)
void oproj(const char* __restrict__ ws, unsigned short* __restrict__ out) {
  __shared__ __attribute__((aligned(16))) float red[8 * 64 * 68];
  const unsigned tid = threadIdx.x, lane = tid & 63u, wid = tid >> 6, l32 = lane & 31u, hi = lane >> 5;
  const unsigned g = blockIdx.x, xc = g & 7u, idx = g >> 3;
  const unsigned qt = 4u * xc + (idx & 3u), nt = idx >> 2;
  const char* ofp = ws + WS_OF + ((size_t)(2u * qt) * 256 + 32u * wid) * 1024 + lane * 16u;
  const char* wfp = ws + WS_WOF + ((size_t)(2u * nt) * 256 + 32u * wid) * 1024 + lane * 16u;
  f32x16 acc[2][2] = {};
  bf16x8 fo[4][2], fw[4][2];
  #pragma unroll
  for (int i = 0; i < 4; ++i) {
    fo[i][0] = *(const bf16x8*)(ofp + i * 1024); fo[i][1] = *(const bf16x8*)(ofp + 262144 + i * 1024);
    fw[i][0] = *(const bf16x8*)(wfp + i * 1024); fw[i][1] = *(const bf16x8*)(wfp + 262144 + i * 1024);
  }
  #pragma unroll 1
  for (int k0 = 0; k0 < 32; k0 += 4) {
    #pragma unroll
    for (int i = 0; i < 4; ++i) {
      const bf16x8 o0 = fo[i][0], o1 = fo[i][1], w0 = fw[i][0], w1 = fw[i][1];
      if (k0 + 4 < 32) {
        const int kn = k0 + 4 + i;
        fo[i][0] = *(const bf16x8*)(ofp + kn * 1024); fo[i][1] = *(const bf16x8*)(ofp + 262144 + kn * 1024);
        fw[i][0] = *(const bf16x8*)(wfp + kn * 1024); fw[i][1] = *(const bf16x8*)(wfp + 262144 + kn * 1024);
      }
      acc[0][0] = __builtin_amdgcn_mfma_f32_32x32x16_bf16(w0, o0, acc[0][0], 0, 0, 0);
      acc[0][1] = __builtin_amdgcn_mfma_f32_32x32x16_bf16(w0, o1, acc[0][1], 0, 0, 0);
      acc[1][0] = __builtin_amdgcn_mfma_f32_32x32x16_bf16(w1, o0, acc[1][0], 0, 0, 0);
      acc[1][1] = __builtin_amdgcn_mfma_f32_32x32x16_bf16(w1, o1, acc[1][1], 0, 0, 0);
    }
  }
  float* rw = red + wid * (64 * 68);
  #pragma unroll
  for (int nb = 0; nb < 2; ++nb)
    #pragma unroll
    for (int qb = 0; qb < 2; ++qb)
      #pragma unroll
      for (int gq = 0; gq < 4; ++gq) {
        f32x4 v = {acc[nb][qb][4 * gq], acc[nb][qb][4 * gq + 1], acc[nb][qb][4 * gq + 2], acc[nb][qb][4 * gq + 3]};
        *(f32x4*)(rw + (32 * qb + l32) * 68 + 32 * nb + 8 * gq + 4 * hi) = v;
      }
  __syncthreads();
  {
    const unsigned qr = tid >> 3, n0 = (tid & 7u) * 8u;
    f32x4 s0 = {}, s1 = {};
    #pragma unroll
    for (int w = 0; w < 8; ++w) {
      s0 += *(const f32x4*)(red + w * (64 * 68) + qr * 68 + n0);
      s1 += *(const f32x4*)(red + w * (64 * 68) + qr * 68 + n0 + 4);
    }
    u32x4 w; w[0] = cvtpk(s0[0], s0[1]); w[1] = cvtpk(s0[2], s0[3]); w[2] = cvtpk(s1[0], s1[1]); w[3] = cvtpk(s1[2], s1[3]);
    *(u32x4*)(out + (size_t)(64u * qt + qr) * DM + 64u * nt + n0) = w;
  }
}

extern "C" void kernel_launch(void* const* d_in, const int* in_sizes, int n_in, void* d_out, int out_size, void* d_ws, size_t ws_size, hipStream_t stream) {
  (void)in_sizes; (void)n_in; (void)out_size; (void)ws_size;
  (void)hipFuncSetAttribute((const void*)attn_fused, hipFuncAttributeMaxDynamicSharedMemorySize, (int)SHM_ATTN);
  hipLaunchKernelGGL(attn_fused, dim3(256), dim3(256), SHM_ATTN, stream, (const unsigned short*)d_in[0], (const unsigned short*)d_in[1], (const unsigned short*)d_in[2],
                     (const unsigned short*)d_in[3], (const unsigned short*)d_in[4], (char*)d_ws);
  hipLaunchKernelGGL(oproj, dim3(256), dim3(512), 0, stream, (const char*)d_ws, (unsigned short*)d_out);
}
```

```cpp
#include <hip/hip_runtime.h>
#include <utility>

constexpr int NB = 8, SQ = 256, DM = 512, NH = 32, SK = 1024, DH = 128, NT = SK / 64;
constexpr float THR = 8.0f;
constexpr size_t SHM_ATTN = 163840;
typedef float    f32x16 __attribute__((ext_vector_type(16)));
typedef float    f32x2  __attribute__((ext_vector_type(2)));
typedef float    f32x4  __attribute__((ext_vector_type(4)));
typedef __bf16   bf16x8 __attribute__((ext_vector_type(8)));
typedef __bf16   bf16x4 __attribute__((ext_vector_type(4)));
typedef __bf16   bf16x2 __attribute__((ext_vector_type(2)));
typedef unsigned u32x4  __attribute__((ext_vector_type(4)));
typedef unsigned u32x2  __attribute__((ext_vector_type(2)));
template <int... Is> using iseq = std::integer_sequence<int, Is...>;
template <int N> using make_iseq = std::make_integer_sequence<int, N>;

#define SLOTB 0x8000u
#define KOFF  0x4000u
#define QBUF(i) (49152u * ((i) % 3))
__device__ __forceinline__ constexpr unsigned tslot(unsigned t) { return ((t + 4u) % 5u) * SLOTB; }
#define QWOFF 32768u
#define SRD_WORD3 0x00020000
#define ST_AUX 16
#define DI __device__ __forceinline__
#define GAP() __builtin_amdgcn_sched_barrier(0)
#define W4_ACLOB "a0","a1","a2","a3","a4","a5","a6","a7","a8","a9","a10","a11","a12","a13","a14","a15","a16","a17","a18","a19","a20","a21","a22","a23","a24","a25","a26","a27","a28","a29","a30","a31","a32","a33","a34","a35","a36","a37","a38","a39","a40","a41","a42","a43","a44","a45","a46","a47","a48","a49","a50","a51","a52","a53","a54","a55","a56","a57","a58","a59","a60","a61","a62","a63","a64","a65","a66","a67","a68","a69","a70","a71","a72","a73","a74","a75","a76","a77","a78","a79","a80","a81","a82","a83","a84","a85","a86","a87","a88","a89","a90","a91","a92","a93","a94","a95","a96","a97","a98","a99","a100","a101","a102","a103","a104","a105","a106","a107","a108","a109","a110","a111","a112","a113","a114","a115","a116","a117","a118","a119","a120","a121","a122","a123","a124","a125","a126","a127"
#define W4_ACLOB_HI "a128","a129","a130","a131","a132","a133","a134","a135","a136","a137","a138","a139","a140","a141","a142","a143","a144","a145","a146","a147","a148","a149","a150","a151","a152","a153","a154","a155","a156","a157","a158","a159","a160","a161","a162","a163","a164","a165","a166","a167","a168","a169","a170","a171","a172","a173","a174","a175","a176","a177","a178","a179","a180","a181","a182","a183","a184","a185","a186","a187","a188","a189","a190","a191","a192","a193","a194","a195","a196","a197","a198","a199","a200","a201","a202","a203","a204","a205","a206","a207","a208","a209","a210","a211","a212","a213","a214","a215","a216","a217","a218","a219","a220","a221","a222","a223","a224","a225","a226","a227","a228","a229","a230","a231","a232","a233","a234","a235","a236","a237","a238","a239","a240","a241","a242","a243","a244","a245","a246","a247","a248","a249","a250","a251","a252","a253","a254","a255"
#define W4_RESCALE_TEXT "v_accvgpr_read_b32 %0, a0\n\tv_mul_f32 %0, %2, %0\n\tv_accvgpr_write_b32 a0, %0\n\t" "v_accvgpr_read_b32 %1, a1\n\tv_mul_f32 %1, %2, %1\n\tv_accvgpr_write_b32 a1, %1\n\t" "v_accvgpr_read_b32 %0, a2\n\tv_mul_f32 %0, %2, %0\n\tv_accvgpr_write_b32 a2, %0\n\t" "v_accvgpr_read_b32 %1, a3\n\tv_mul_f32 %1, %2, %1\n\tv_accvgpr_write_b32 a3, %1\n\t" "v_accvgpr_read_b32 %0, a4\n\tv_mul_f32 %0, %2, %0\n\tv_accvgpr_write_b32 a4, %0\n\t" "v_accvgpr_read_b32 %1, a5\n\tv_mul_f32 %1, %2, %1\n\tv_accvgpr_write_b32 a5, %1\n\t" "v_accvgpr_read_b32 %0, a6\n\tv_mul_f32 %0, %2, %0\n\tv_accvgpr_write_b32 a6, %0\n\t" "v_accvgpr_read_b32 %1, a7\n\tv_mul_f32 %1, %2, %1\n\tv_accvgpr_write_b32 a7, %1\n\t" "v_accvgpr_read_b32 %0, a8\n\tv_mul_f32 %0, %2, %0\n\tv_accvgpr_write_b32 a8, %0\n\t" "v_accvgpr_read_b32 %1, a9\n\tv_mul_f32 %1, %2, %1\n\tv_accvgpr_write_b32 a9, %1\n\t" "v_accvgpr_read_b32 %0, a10\n\tv_mul_f32 %0, %2, %0\n\tv_accvgpr_write_b32 a10, %0\n\t" "v_accvgpr_read_b32 %1, a11\n\tv_mul_f32 %1, %2, %1\n\tv_accvgpr_write_b32 a11, %1\n\t" "v_accvgpr_read_b32 %0, a12\n\tv_mul_f32 %0, %2, %0\n\tv_accvgpr_write_b32 a12, %0\n\t" "v_accvgpr_read_b32 %1, a13\n\tv_mul_f32 %1, %2, %1\n\tv_accvgpr_write_b32 a13, %1\n\t" "v_accvgpr_read_b32 %0, a14\n\tv_mul_f32 %0, %2, %0\n\tv_accvgpr_write_b32 a14, %0\n\t" "v_accvgpr_read_b32 %1, a15\n\tv_mul_f32 %1, %2, %1\n\tv_accvgpr_write_b32 a15, %1\n\t" "v_accvgpr_read_b32 %0, a16\n\tv_mul_f32 %0, %3, %0\n\tv_accvgpr_write_b32 a16, %0\n\t" "v_accvgpr_read_b32 %1, a17\n\tv_mul_f32 %1, %3, %1\n\tv_accvgpr_write_b32 a17, %1\n\t" "v_accvgpr_read_b32 %0, a18\n\tv_mul_f32 %0, %3, %0\n\tv_accvgpr_write_b32 a18, %0\n\t" "v_accvgpr_read_b32 %1, a19\n\tv_mul_f32 %1, %3, %1\n\tv_accvgpr_write_b32 a19, %1\n\t" "v_accvgpr_read_b32 %0, a20\n\tv_mul_f32 %0, %3, %0\n\tv_accvgpr_write_b32 a20, %0\n\t" "v_accvgpr_read_b32 %1, a21\n\tv_mul_f32 %1, %3, %1\n\tv_accvgpr_write_b32 a21, %1\n\t" "v_accvgpr_read_b32 %0, a22\n\tv_mul_f32 %0, %3, %0\n\tv_accvgpr_write_b32 a22, %0\n\t" "v_accvgpr_read_b32 %1, a23\n\tv_mul_f32 %1, %3, %1\n\tv_accvgpr_write_b32 a23, %1\n\t" "v_accvgpr_read_b32 %0, a24\n\tv_mul_f32 %0, %3, %0\n\tv_accvgpr_write_b32 a24, %0\n\t" "v_accvgpr_read_b32 %1, a25\n\tv_mul_f32 %1, %3, %1\n\tv_accvgpr_write_b32 a25, %1\n\t" "v_accvgpr_read_b32 %0, a26\n\tv_mul_f32 %0, %3, %0\n\tv_accvgpr_write_b32 a26, %0\n\t" "v_accvgpr_read_b32 %1, a27\n\tv_mul_f32 %1, %3, %1\n\tv_accvgpr_write_b32 a27, %1\n\t" "v_accvgpr_read_b32 %0, a28\n\tv_mul_f32 %0, %3, %0\n\tv_accvgpr_write_b32 a28, %0\n\t" "v_accvgpr_read_b32 %1, a29\n\tv_mul_f32 %1, %3, %1\n\tv_accvgpr_write_b32 a29, %1\n\t" "v_accvgpr_read_b32 %0, a30\n\tv_mul_f32 %0, %3, %0\n\tv_accvgpr_write_b32 a30, %0\n\t" "v_accvgpr_read_b32 %1, a31\n\tv_mul_f32 %1, %3, %1\n\tv_accvgpr_write_b32 a31, %1\n\t" "v_accvgpr_read_b32 %0, a32\n\tv_mul_f32 %0, %2, %0\n\tv_accvgpr_write_b32 a32, %0\n\t" "v_accvgpr_read_b32 %1, a33\n\tv_mul_f32 %1, %2, %1\n\tv_accvgpr_write_b32 a33, %1\n\t" "v_accvgpr_read_b32 %0, a34\n\tv_mul_f32 %0, %2, %0\n\tv_accvgpr_write_b32 a34, %0\n\t" "v_accvgpr_read_b32 %1, a35\n\tv_mul_f32 %1, %2, %1\n\tv_accvgpr_write_b32 a35, %1\n\t" "v_accvgpr_read_b32 %0, a36\n\tv_mul_f32 %0, %2, %0\n\tv_accvgpr_write_b32 a36, %0\n\t" "v_accvgpr_read_b32 %1, a37\n\tv_mul_f32 %1, %2, %1\n\tv_accvgpr_write_b32 a37, %1\n\t" "v_accvgpr_read_b32 %0, a38\n\tv_mul_f32 %0, %2, %0\n\tv_accvgpr_write_b32 a38, %0\n\t" "v_accvgpr_read_b32 %1, a39\n\tv_mul_f32 %1, %2, %1\n\tv_accvgpr_write_b32 a39, %1\n\t" "v_accvgpr_read_b32 %0, a40\n\tv_mul_f32 %0, %2, %0\n\tv_accvgpr_write_b32 a40, %0\n\t" "v_accvgpr_read_b32 %1, a41\n\tv_mul_f32 %1, %2, %1\n\tv_accvgpr_write_b32 a41, %1\n\t" "v_accvgpr_read_b32 %0, a42\n\tv_mul_f32 %0, %2, %0\n\tv_accvgpr_write_b32 a42, %0\n\t" "v_accvgpr_read_b32 %1, a43\n\tv_mul_f32 %1, %2, %1\n\tv_accvgpr_write_b32 a43, %1\n\t" "v_accvgpr_read_b32 %0, a44\n\tv_mul_f32 %0, %2, %0\n\tv_accvgpr_write_b32 a44, %0\n\t" "v_accvgpr_read_b32 %1, a45\n\tv_mul_f32 %1, %2, %1\n\tv_accvgpr_write_b32 a45, %1\n\t" "v_accvgpr_read_b32 %0, a46\n\tv_mul_f32 %0, %2, %0\n\tv_accvgpr_write_b32 a46, %0\n\t" "v_accvgpr_read_b32 %1, a47\n\tv_mul_f32 %1, %2, %1\n\tv_accvgpr_write_b32 a47, %1\n\t" "v_accvgpr_read_b32 %0, a48\n\tv_mul_f32 %0, %3, %0\n\tv_accvgpr_write_b32 a48, %0\n\t" "v_accvgpr_read_b32 %1, a49\n\tv_mul_f32 %1, %3, %1\n\tv_accvgpr_write_b32 a49, %1\n\t" "v_accvgpr_read_b32 %0, a50\n\tv_mul_f32 %0, %3, %0\n\tv_accvgpr_write_b32 a50, %0\n\t" "v_accvgpr_read_b32 %1, a51\n\tv_mul_f32 %1, %3, %1\n\tv_accvgpr_write_b32 a51, %1\n\t" "v_accvgpr_read_b32 %0, a52\n\tv_mul_f32 %0, %3, %0\n\tv_accvgpr_write_b32 a52, %0\n\t" "v_accvgpr_read_b32 %1, a53\n\tv_mul_f32 %1, %3, %1\n\tv_accvgpr_write_b32 a53, %1\n\t" "v_accvgpr_read_b32 %0, a54\n\tv_mul_f32 %0, %3, %0\n\tv_accvgpr_write_b32 a54, %0\n\t" "v_accvgpr_read_b32 %1, a55\n\tv_mul_f32 %1, %3, %1\n\tv_accvgpr_write_b32 a55, %1\n\t" "v_accvgpr_read_b32 %0, a56\n\tv_mul_f32 %0, %3, %0\n\tv_accvgpr_write_b32 a56, %0\n\t" "v_accvgpr_read_b32 %1, a57\n\tv_mul_f32 %1, %3, %1\n\tv_accvgpr_write_b32 a57, %1\n\t" "v_accvgpr_read_b32 %0, a58\n\tv_mul_f32 %0, %3, %0\n\tv_accvgpr_write_b32 a58, %0\n\t" "v_accvgpr_read_b32 %1, a59\n\tv_mul_f32 %1, %3, %1\n\tv_accvgpr_write_b32 a59, %1\n\t" "v_accvgpr_read_b32 %0, a60\n\tv_mul_f32 %0, %3, %0\n\tv_accvgpr_write_b32 a60, %0\n\t" "v_accvgpr_read_b32 %1, a61\n\tv_mul_f32 %1, %3, %1\n\tv_accvgpr_write_b32 a61, %1\n\t" "v_accvgpr_read_b32 %0, a62\n\tv_mul_f32 %0, %3, %0\n\tv_accvgpr_write_b32 a62, %0\n\t" "v_accvgpr_read_b32 %1, a63\n\tv_mul_f32 %1, %3, %1\n\tv_accvgpr_write_b32 a63, %1\n\t" "v_accvgpr_read_b32 %0, a64\n\tv_mul_f32 %0, %2, %0\n\tv_accvgpr_write_b32 a64, %0\n\t" "v_accvgpr_read_b32 %1, a65\n\tv_mul_f32 %1, %2, %1\n\tv_accvgpr_write_b32 a65, %1\n\t" "v_accvgpr_read_b32 %0, a66\n\tv_mul_f32 %0, %2, %0\n\tv_accvgpr_write_b32 a66, %0\n\t" "v_accvgpr_read_b32 %1, a67\n\tv_mul_f32 %1, %2, %1\n\tv_accvgpr_write_b32 a67, %1\n\t" "v_accvgpr_read_b32 %0, a68\n\tv_mul_f32 %0, %2, %0\n\tv_accvgpr_write_b32 a68, %0\n\t" "v_accvgpr_read_b32 %1, a69\n\tv_mul_f32 %1, %2, %1\n\tv_accvgpr_write_b32 a69, %1\n\t" "v_accvgpr_read_b32 %0, a70\n\tv_mul_f32 %0, %2, %0\n\tv_accvgpr_write_b32 a70, %0\n\t" "v_accvgpr_read_b32 %1, a71\n\tv_mul_f32 %1, %2, %1\n\tv_accvgpr_write_b32 a71, %1\n\t" "v_accvgpr_read_b32 %0, a72\n\tv_mul_f32 %0, %2, %0\n\tv_accvgpr_write_b32 a72, %0\n\t" "v_accvgpr_read_b32 %1, a73\n\tv_mul_f32 %1, %2, %1\n\tv_accvgpr_write_b32 a73, %1\n\t" "v_accvgpr_read_b32 %0, a74\n\tv_mul_f32 %0, %2, %0\n\tv_accvgpr_write_b32 a74, %0\n\t" "v_accvgpr_read_b32 %1, a75\n\tv_mul_f32 %1, %2, %1\n\tv_accvgpr_write_b32 a75, %1\n\t" "v_accvgpr_read_b32 %0, a76\n\tv_mul_f32 %0, %2, %0\n\tv_accvgpr_write_b32 a76, %0\n\t" "v_accvgpr_read_b32 %1, a77\n\tv_mul_f32 %1, %2, %1\n\tv_accvgpr_write_b32 a77, %1\n\t" "v_accvgpr_read_b32 %0, a78\n\tv_mul_f32 %0, %2, %0\n\tv_accvgpr_write_b32 a78, %0\n\t" "v_accvgpr_read_b32 %1, a79\n\tv_mul_f32 %1, %2, %1\n\tv_accvgpr_write_b32 a79, %1\n\t" "v_accvgpr_read_b32 %0, a80\n\tv_mul_f32 %0, %3, %0\n\tv_accvgpr_write_b32 a80, %0\n\t" "v_accvgpr_read_b32 %1, a81\n\tv_mul_f32 %1, %3, %1\n\tv_accvgpr_write_b32 a81, %1\n\t" "v_accvgpr_read_b32 %0, a82\n\tv_mul_f32 %0, %3, %0\n\tv_accvgpr_write_b32 a82, %0\n\t" "v_accvgpr_read_b32 %1, a83\n\tv_mul_f32 %1, %3, %1\n\tv_accvgpr_write_b32 a83, %1\n\t" "v_accvgpr_read_b32 %0, a84\n\tv_mul_f32 %0, %3, %0\n\tv_accvgpr_write_b32 a84, %0\n\t" "v_accvgpr_read_b32 %1, a85\n\tv_mul_f32 %1, %3, %1\n\tv_accvgpr_write_b32 a85, %1\n\t" "v_accvgpr_read_b32 %0, a86\n\tv_mul_f32 %0, %3, %0\n\tv_accvgpr_write_b32 a86, %0\n\t" "v_accvgpr_read_b32 %1, a87\n\tv_mul_f32 %1, %3, %1\n\tv_accvgpr_write_b32 a87, %1\n\t" "v_accvgpr_read_b32 %0, a88\n\tv_mul_f32 %0, %3, %0\n\tv_accvgpr_write_b32 a88, %0\n\t" "v_accvgpr_read_b32 %1, a89\n\tv_mul_f32 %1, %3, %1\n\tv_accvgpr_write_b32 a89, %1\n\t" "v_accvgpr_read_b32 %0, a90\n\tv_mul_f32 %0, %3, %0\n\tv_accvgpr_write_b32 a90, %0\n\t" "v_accvgpr_read_b32 %1, a91\n\tv_mul_f32 %1, %3, %1\n\tv_accvgpr_write_b32 a91, %1\n\t" "v_accvgpr_read_b32 %0, a92\n\tv_mul_f32 %0, %3, %0\n\tv_accvgpr_write_b32 a92, %0\n\t" "v_accvgpr_read_b32 %1, a93\n\tv_mul_f32 %1, %3, %1\n\tv_accvgpr_write_b32 a93, %1\n\t" "v_accvgpr_read_b32 %0, a94\n\tv_mul_f32 %0, %3, %0\n\tv_accvgpr_write_b32 a94, %0\n\t" "v_accvgpr_read_b32 %1, a95\n\tv_mul_f32 %1, %3, %1\n\tv_accvgpr_write_b32 a95, %1\n\t" "v_accvgpr_read_b32 %0, a96\n\tv_mul_f32 %0, %2, %0\n\tv_accvgpr_write_b32 a96, %0\n\t" "v_accvgpr_read_b32 %1, a97\n\tv_mul_f32 %1, %2, %1\n\tv_accvgpr_write_b32 a97, %1\n\t" "v_accvgpr_read_b32 %0, a98\n\tv_mul_f32 %0, %2, %0\n\tv_accvgpr_write_b32 a98, %0\n\t" "v_accvgpr_read_b32 %1, a99\n\tv_mul_f32 %1, %2, %1\n\tv_accvgpr_write_b32 a99, %1\n\t" "v_accvgpr_read_b32 %0, a100\n\tv_mul_f32 %0, %2, %0\n\tv_accvgpr_write_b32 a100, %0\n\t" "v_accvgpr_read_b32 %1, a101\n\tv_mul_f32 %1, %2, %1\n\tv_accvgpr_write_b32 a101, %1\n\t" "v_accvgpr_read_b32 %0, a102\n\tv_mul_f32 %0, %2, %0\n\tv_accvgpr_write_b32 a102, %0\n\t" "v_accvgpr_read_b32 %1, a103\n\tv_mul_f32 %1, %2, %1\n\tv_accvgpr_write_b32 a103, %1\n\t" "v_accvgpr_read_b32 %0, a104\n\tv_mul_f32 %0, %2, %0\n\tv_accvgpr_write_b32 a104, %0\n\t" "v_accvgpr_read_b32 %1, a105\n\tv_mul_f32 %1, %2, %1\n\tv_accvgpr_write_b32 a105, %1\n\t" "v_accvgpr_read_b32 %0, a106\n\tv_mul_f32 %0, %2, %0\n\tv_accvgpr_write_b32 a106, %0\n\t" "v_accvgpr_read_b32 %1, a107\n\tv_mul_f32 %1, %2, %1\n\tv_accvgpr_write_b32 a107, %1\n\t" "v_accvgpr_read_b32 %0, a108\n\tv_mul_f32 %0, %2, %0\n\tv_accvgpr_write_b32 a108, %0\n\t" "v_accvgpr_read_b32 %1, a109\n\tv_mul_f32 %1, %2, %1\n\tv_accvgpr_write_b32 a109, %1\n\t" "v_accvgpr_read_b32 %0, a110\n\tv_mul_f32 %0, %2, %0\n\tv_accvgpr_write_b32 a110, %0\n\t" "v_accvgpr_read_b32 %1, a111\n\tv_mul_f32 %1, %2, %1\n\tv_accvgpr_write_b32 a111, %1\n\t" "v_accvgpr_read_b32 %0, a112\n\tv_mul_f32 %0, %3, %0\n\tv_accvgpr_write_b32 a112, %0\n\t" "v_accvgpr_read_b32 %1, a113\n\tv_mul_f32 %1, %3, %1\n\tv_accvgpr_write_b32 a113, %1\n\t" "v_accvgpr_read_b32 %0, a114\n\tv_mul_f32 %0, %3, %0\n\tv_accvgpr_write_b32 a114, %0\n\t" "v_accvgpr_read_b32 %1, a115\n\tv_mul_f32 %1, %3, %1\n\tv_accvgpr_write_b32 a115, %1\n\t" "v_accvgpr_read_b32 %0, a116\n\tv_mul_f32 %0, %3, %0\n\tv_accvgpr_write_b32 a116, %0\n\t" "v_accvgpr_read_b32 %1, a117\n\tv_mul_f32 %1, %3, %1\n\tv_accvgpr_write_b32 a117, %1\n\t" "v_accvgpr_read_b32 %0, a118\n\tv_mul_f32 %0, %3, %0\n\tv_accvgpr_write_b32 a118, %0\n\t" "v_accvgpr_read_b32 %1, a119\n\tv_mul_f32 %1, %3, %1\n\tv_accvgpr_write_b32 a119, %1\n\t" "v_accvgpr_read_b32 %0, a120\n\tv_mul_f32 %0, %3, %0\n\tv_accvgpr_write_b32 a120, %0\n\t" "v_accvgpr_read_b32 %1, a121\n\tv_mul_f32 %1, %3, %1\n\tv_accvgpr_write_b32 a121, %1\n\t" "v_accvgpr_read_b32 %0, a122\n\tv_mul_f32 %0, %3, %0\n\tv_accvgpr_write_b32 a122, %0\n\t" "v_accvgpr_read_b32 %1, a123\n\tv_mul_f32 %1, %3, %1\n\tv_accvgpr_write_b32 a123, %1\n\t" "v_accvgpr_read_b32 %0, a124\n\tv_mul_f32 %0, %3, %0\n\tv_accvgpr_write_b32 a124, %0\n\t" "v_accvgpr_read_b32 %1, a125\n\tv_mul_f32 %1, %3, %1\n\tv_accvgpr_write_b32 a125, %1\n\t" "v_accvgpr_read_b32 %0, a126\n\tv_mul_f32 %0, %3, %0\n\tv_accvgpr_write_b32 a126, %0\n\t" "v_accvgpr_read_b32 %1, a127\n\tv_mul_f32 %1, %3, %1\n\tv_accvgpr_write_b32 a127, %1\n\t"


DI unsigned rfl(unsigned x) { return __builtin_amdgcn_readfirstlane(x); }
DI unsigned cvtpk(float lo, float hi) {
  f32x2 v = {lo, hi}; bf16x2 b = __builtin_convertvector(v, bf16x2); return __builtin_bit_cast(unsigned, b);
}
DI float max3f(float a, float b, float c) { float r; asm("v_max3_f32 %0, %1, %2, %3" : "=v"(r) : "v"(a), "v"(b), "v"(c)); return r; }
DI float max2f(float a, float b) { float r; asm("v_max_f32_e32 %0, %1, %2" : "=v"(r) : "v"(a), "v"(b)); return r; }
DI float fadd_s(float a, float b) { float r; asm("v_add_f32_e32 %0, %1, %2" : "=v"(r) : "v"(a), "v"(b)); return r; }
DI float fmul_s(float a, float b) { float r; asm("v_mul_f32_e32 %0, %1, %2" : "=v"(r) : "v"(a), "v"(b)); return r; }
DI float fsub_s(float a, float b) { float r; asm("v_sub_f32_e32 %0, %1, %2" : "=v"(r) : "v"(a), "v"(b)); return r; }
DI float w4_aone() { unsigned l; asm volatile("v_mbcnt_lo_u32_b32 %0, -1, 0\n\tv_mbcnt_hi_u32_b32 %0, -1, %0" : "=v"(l)); return l < 32u ? 1.f : 0.f; }
DI void negm_bcast(f32x16& negm, float aone, float neg_mhat) {
  asm volatile("s_nop 1\n\tv_mfma_f32_32x32x2_f32 %0, %1, %2, 0" : "+v"(negm) : "v"(aone), "v"(neg_mhat));
}

struct W4C {
  unsigned vkb[4], vvb, vksrc[2], vvsrc, m0k, m0v, sofk, sofv, ldsb;
  __amdgpu_buffer_rsrc_t srdK, srdV;
  f32x16 S[2][2][2];
  f32x16 NEGM[2];
  bf16x8 Vf[4][4];
  bf16x8 Pk[2][4];
  bf16x4 vlo[4][4], vup[4][4];
  float mhat[2], ls[2][2], fct[2]; bool pend;
  float pmx0[2], pmx1[2], rmax[2];
  unsigned vvsrc2[2], sofv2, sofk3;
};

constexpr int AO_(int db, int qb) { return 16 * (2 * db + qb); }
constexpr int AQ_(int qb, int d0) { return 128 + 32 * qb + 4 * d0; }
constexpr int AK_(int kb, int d0) { return 192 + 32 * kb + 4 * d0; }

template <int I> DI void tr_read(W4C& c, unsigned vslot) {
  constexpr int ks = I >> 3, db = (I >> 1) & 3;
  if constexpr ((I & 1) == 0) asm volatile("ds_read_b64_tr_b16 %0, %1 offset:%2" : "=v"(c.vlo[db][ks]) : "v"(c.vvb + vslot), "i"(4096 * ks + 512 * db) : "memory");
  else                        asm volatile("ds_read_b64_tr_b16 %0, %1 offset:%2" : "=v"(c.vup[db][ks]) : "v"(c.vvb + vslot), "i"(4096 * ks + 2048 + 512 * db) : "memory");
}
template <int I> DI void k_read(W4C& c, unsigned slot) {
  constexpr int kb = I >> 3, d0 = I & 7;
  asm volatile("ds_read_b128 a[%c1:%c2], %0 offset:%c3" :: "v"(c.vkb[d0 & 3] + slot), "i"(AK_(kb, d0)), "i"(AK_(kb, d0) + 3), "i"(8192 * kb + 128 * (d0 >> 2)) : "memory", W4_ACLOB_HI);
}
template <int X, bool FIRST, int M> DI void qk_mfma(W4C& c) {
  constexpr int d0 = M >> 2, kb = (M >> 1) & 1, qb = M & 1, ak = AK_(kb, d0), aq = AQ_(qb, d0);
  if constexpr (d0 == 0) {
    if constexpr (FIRST) asm volatile("v_mfma_f32_32x32x16_bf16 %0, a[%c1:%c2], a[%c3:%c4], 0" : "=&v"(c.S[X][kb][qb]) : "i"(ak), "i"(ak + 3), "i"(aq), "i"(aq + 3) : W4_ACLOB, W4_ACLOB_HI);
    else                 asm volatile("v_mfma_f32_32x32x16_bf16 %0, a[%c2:%c3], a[%c4:%c5], %1" : "=&v"(c.S[X][kb][qb]) : "v"(c.NEGM[qb]), "i"(ak), "i"(ak + 3), "i"(aq), "i"(aq + 3) : W4_ACLOB, W4_ACLOB_HI);
  } else asm volatile("v_mfma_f32_32x32x16_bf16 %0, a[%c1:%c2], a[%c3:%c4], %0" : "+v"(c.S[X][kb][qb]) : "i"(ak), "i"(ak + 3), "i"(aq), "i"(aq + 3) : W4_ACLOB, W4_ACLOB_HI);
}
template <int M, bool ZERO> DI void pv_mfma(W4C& c) {
  constexpr int ks = M >> 3, db = (M >> 1) & 3, qb = M & 1, ao = AO_(db, qb);
  if constexpr (ZERO && ks == 0) asm volatile("v_mfma_f32_32x32x16_bf16 a[%c2:%c3], %0, %1, 0" :: "v"(c.Vf[db][ks]), "v"(c.Pk[qb][ks]), "i"(ao), "i"(ao + 15) : W4_ACLOB);
  else                         asm volatile("v_mfma_f32_32x32x16_bf16 a[%c2:%c3], %0, %1, a[%c2:%c3]" :: "v"(c.Vf[db][ks]), "v"(c.Pk[qb][ks]), "i"(ao), "i"(ao + 15) : W4_ACLOB);
}
constexpr int E_kb(int e) { return e >> 5; }
constexpr int E_q(int e)  { return (e >> 4) & 1; }
constexpr int E_r(int e)  { return e & 15; }
template <int X, int E> DI void exp1(W4C& c) { c.S[X][E_kb(E)][E_q(E)][E_r(E)] = __builtin_amdgcn_exp2f(c.S[X][E_kb(E)][E_q(E)][E_r(E)]); }
template <int X, int E> DI void add1(W4C& c) { c.ls[E_q(E)][E & 1] = fadd_s(c.ls[E_q(E)][E & 1], c.S[X][E_kb(E)][E_q(E)][E_r(E)]); }
template <int X, int P> DI void cvt1(W4C& c) {
  constexpr int e = 2 * P, kb = E_kb(e), q = E_q(e), r = E_r(e), ks = 2 * kb + (r >> 3), d = (r & 7) >> 1;
  u32x4 w = __builtin_bit_cast(u32x4, c.Pk[q][ks]);
  w[d] = cvtpk(c.S[X][kb][q][r], c.S[X][kb][q][r + 1]);
  c.Pk[q][ks] = __builtin_bit_cast(bf16x8, w);
  asm volatile("" :: "v"(w[d]));
}
template <bool FIRST> DI void max_group(float& m0, float& m1, const f32x16& s0, const f32x16& s1, const int r) {
  if constexpr (FIRST) asm("v_max3_f32 %0, %2, %3, %6\n\tv_max3_f32 %1, %4, %5, %7\n\tv_max3_f32 %0, %0, %8, %9" : "=&v"(m0), "=&v"(m1)
                           : "v"(s0[r]), "v"(s0[r + 1]), "v"(s0[r + 2]), "v"(s0[r + 3]), "v"(s1[r]), "v"(s1[r + 1]), "v"(s1[r + 2]), "v"(s1[r + 3]));
  else asm("v_max3_f32 %0, %0, %2, %3\n\tv_max3_f32 %1, %1, %4, %5\n\tv_max3_f32 %0, %0, %6, %7\n\tv_max3_f32 %1, %1, %8, %9" : "+v"(m0), "+v"(m1)
           : "v"(s0[r]), "v"(s0[r + 1]), "v"(s0[r + 2]), "v"(s0[r + 3]), "v"(s1[r]), "v"(s1[r + 1]), "v"(s1[r + 2]), "v"(s1[r + 3]));
}
#define SET_M0(V) asm volatile("s_mov_b32 m0, %0" :: "s"(V) : "m0", "memory")
#define DMA_OFF_NT(VOFFV, SRD, SOFF, OFF) asm volatile("buffer_load_dwordx4 %0, %1, %2 offen offset:%3 nt lds" :: "v"(VOFFV), "s"(SRD), "s"(SOFF), "i"(OFF) : "memory")

template <int X, bool DMA, int M> DI void gapA(W4C& c, unsigned vslot, unsigned dm0, unsigned dso) {
  constexpr int Xp = X ^ 1;
  if constexpr (DMA && M >= 24 && (M & 1) == 0) SET_M0(dm0);
  qk_mfma<X, false, M>(c);
  GAP();
  if constexpr (DMA && M >= 24 && (M & 1) == 0) { constexpr int q = (M - 24) >> 1; DMA_OFF_NT(c.vvsrc2[q & 1], c.srdV, dso, 1024 * q); }
  if constexpr (M == 8) asm volatile("" :: "v"(c.NEGM[0]), "v"(c.NEGM[1]));
  if constexpr (M < 12) { tr_read<M>(c, vslot); exp1<Xp, 40 + 2 * M>(c); cvt1<Xp, M>(c); exp1<Xp, 41 + 2 * M>(c); }
  else if constexpr (M < 22) { constexpr int a0 = 18 + 3 * (M - 12);
    tr_read<12 + 2 * (M - 12)>(c, vslot); add1<Xp, a0>(c); add1<Xp, a0 + 1>(c); cvt1<Xp, M>(c); tr_read<13 + 2 * (M - 12)>(c, vslot); add1<Xp, a0 + 2>(c); }
  else { cvt1<Xp, M>(c); if constexpr (M < 30) { add1<Xp, 48 + 2 * (M - 22)>(c); add1<Xp, 49 + 2 * (M - 22)>(c); } }
  GAP();
}
#define W4_VLO "+v"(c.vlo[0][0]), "+v"(c.vlo[0][1]), "+v"(c.vlo[0][2]), "+v"(c.vlo[0][3]), "+v"(c.vlo[1][0]), "+v"(c.vlo[1][1]), "+v"(c.vlo[1][2]), "+v"(c.vlo[1][3]), \
               "+v"(c.vlo[2][0]), "+v"(c.vlo[2][1]), "+v"(c.vlo[2][2]), "+v"(c.vlo[2][3]), "+v"(c.vlo[3][0]), "+v"(c.vlo[3][1]), "+v"(c.vlo[3][2]), "+v"(c.vlo[3][3])
#define W4_VUP "+v"(c.vup[0][0]), "+v"(c.vup[0][1]), "+v"(c.vup[0][2]), "+v"(c.vup[0][3]), "+v"(c.vup[1][0]), "+v"(c.vup[1][1]), "+v"(c.vup[1][2]), "+v"(c.vup[1][3]), \
               "+v"(c.vup[2][0]), "+v"(c.vup[2][1]), "+v"(c.vup[2][2]), "+v"(c.vup[2][3]), "+v"(c.vup[3][0]), "+v"(c.vup[3][1]), "+v"(c.vup[3][2]), "+v"(c.vup[3][3])
template <int X, bool DMA, int VM, int... Ms> DI void phaseA_(W4C& c, unsigned sl_m1, unsigned sl_p2, unsigned tb, iseq<Ms...>) {
  const unsigned dm0 = c.m0v + sl_p2, dso = tb + c.sofv2;
  (gapA<X, DMA, Ms>(c, sl_m1, dm0, dso), ...);
  asm volatile("s_waitcnt vmcnt(%16) lgkmcnt(0)" : W4_VLO : "i"(VM) : "memory"); asm volatile("s_barrier" : W4_VUP :: "memory");
  GAP();
  #pragma unroll
  for (int ks = 0; ks < 4; ++ks)
    #pragma unroll
    for (int db = 0; db < 4; ++db) c.Vf[db][ks] = __builtin_shufflevector(c.vlo[db][ks], c.vup[db][ks], 0, 1, 2, 3, 4, 5, 6, 7);
}
template <int X, bool DMA, int VM> DI void phaseA(W4C& c, unsigned sl_m1, unsigned sl_p2, unsigned tb) { phaseA_<X, DMA, VM>(c, sl_m1, sl_p2, tb, make_iseq<32>{}); }

template <int X, bool PEEL, bool ZERO1, bool DMA, bool KR, int M> DI void gapB(W4C& c, const unsigned kslot_rd, unsigned dm0, unsigned dso) {
  if constexpr (DMA && (M == 0 || M == 12 || M == 13)) SET_M0(dm0);
  if constexpr (!PEEL) pv_mfma<M, ZERO1>(c); else asm volatile("s_nop 0");
  GAP();
  if constexpr (DMA && M == 0) { DMA_OFF_NT(c.vksrc[0], c.srdK, dso, 0); DMA_OFF_NT(c.vksrc[1], c.srdK, dso, 1024); }
  if constexpr (DMA && M == 12) DMA_OFF_NT(c.vksrc[0], c.srdK, dso, 2048);
  if constexpr (DMA && M == 13) DMA_OFF_NT(c.vksrc[1], c.srdK, dso, 3072);
  if constexpr (M >= 1 && M <= 8) {
    constexpr int i = M - 1, q = i >> 2, r = 4 * (i & 3);
    if constexpr (KR) k_read<2 * i>(c, kslot_rd);
    max_group<(i & 3) == 0>(c.pmx0[q], c.pmx1[q], c.S[X][0][q], c.S[X][1][q], r);
    if constexpr (KR) k_read<2 * i + 1>(c, kslot_rd);
  }
  if constexpr (M == 9 || M == 10) {
    constexpr int q = M - 9; const float mm = max2f(c.pmx0[q], c.pmx1[q]);
    auto pr = __builtin_amdgcn_permlane32_swap(__float_as_uint(mm), __float_as_uint(mm), false, false);
    c.rmax[q] = max2f(__uint_as_float(pr[0]), __uint_as_float(pr[1]));
  }
  if constexpr (M == 11) {
    if constexpr (PEEL) {
      #pragma unroll
      for (int q = 0; q < 2; ++q) { c.mhat[q] = c.rmax[q];
        #pragma unroll
        for (int kb = 0; kb < 2; ++kb) { _Pragma("unroll") for (int e = 0; e < 16; ++e) c.S[X][kb][q][e] = fsub_s(c.S[X][kb][q][e], c.rmax[q]); }
        negm_bcast(c.NEGM[q], w4_aone(), -c.mhat[q]);
      }
    } else {
      const bool grew = max2f(c.rmax[0], c.rmax[1]) > THR;
      if (__builtin_expect(__any((int)grew), 0)) {
        #pragma unroll
        for (int q = 0; q < 2; ++q) { const float dl = max2f(c.rmax[q], 0.f); c.mhat[q] = fadd_s(c.mhat[q], dl); c.fct[q] = __builtin_amdgcn_exp2f(-dl);
          #pragma unroll
          for (int kb = 0; kb < 2; ++kb) { _Pragma("unroll") for (int e = 0; e < 16; ++e) c.S[X][kb][q][e] = fsub_s(c.S[X][kb][q][e], dl); }
          negm_bcast(c.NEGM[q], w4_aone(), -c.mhat[q]);
          asm volatile("s_nop 0" : "+v"(c.fct[q]));
          c.ls[q][0] = fmul_s(c.ls[q][0], c.fct[q]); c.ls[q][1] = fmul_s(c.ls[q][1], c.fct[q]);
        }
        c.pend = true;
      }
    }
  }
  if constexpr (M >= 12) {
    exp1<X, 2 * (M - 12)>(c);
    if constexpr (M >= 14) add1<X, M - 14>(c);
    exp1<X, 2 * (M - 12) + 1>(c);
  }
  if constexpr (M == 31) asm volatile("" :: "v"(c.S[X][0][0]), "v"(c.S[X][0][1]), "v"(c.S[X][1][0]), "v"(c.NEGM[0]), "v"(c.NEGM[1]), "v"(c.ls[0][0]), "v"(c.ls[0][1]), "v"(c.ls[1][0]), "v"(c.ls[1][1]));
  GAP();
}
template <int X, bool PEEL, bool ZERO1, bool DMA, bool KR, int... Ms> DI void phaseB_(W4C& c, unsigned sl_p1, unsigned sl_p3, unsigned tb, iseq<Ms...>) {
  const unsigned dm0 = c.m0k + sl_p3, dso = tb + c.sofk3;
  if constexpr (!PEEL) asm volatile("s_nop 1" ::: "memory");
  GAP();
  (gapB<X, PEEL, ZERO1, DMA, KR, Ms>(c, sl_p1, dm0, dso), ...);
  if constexpr (!PEEL) {
    if (__builtin_expect(c.pend, 0)) {
      float t0, t1;
      asm volatile("s_nop 7\n\ts_nop 7\n\t" W4_RESCALE_TEXT "s_nop 1" : "=&v"(t0), "=&v"(t1) : "v"(c.fct[0]), "v"(c.fct[1]) : W4_ACLOB);
      c.pend = false;
    }
  }
  asm volatile("s_waitcnt lgkmcnt(0)" ::: "memory");
  GAP();
}
template <int X, bool PEEL, bool ZERO1, bool DMA, bool KR> DI void phaseB(W4C& c, unsigned sl_p1, unsigned sl_p3, unsigned tb) { phaseB_<X, PEEL, ZERO1, DMA, KR>(c, sl_p1, sl_p3, tb, make_iseq<32>{}); }

template <int... Is> DI void tr_reads(W4C& c, unsigned vslot, iseq<Is...>) { (tr_read<Is>(c, vslot), ...); }
template <int X, int... Es> DI void exps_from40(W4C& c, iseq<Es...>) { (exp1<X, 40 + Es>(c), ...); }
template <int X, int... Ps> DI void cvts(W4C& c, iseq<Ps...>) { (cvt1<X, Ps>(c), ...); }
template <int X, int... Es> DI void adds_from18(W4C& c, iseq<Es...>) { (add1<X, 18 + Es>(c), ...); }

struct QC {
  unsigned vxb[4], vwb, vxsrc[2], vwsrc, m0x, m0w, sofx, sofw;
  __amdgpu_buffer_rsrc_t srdX, srdW, srdO;
  unsigned vwo, sofo, wo[32];
  bf16x4 alo[2][4], aup[2][4];
  bf16x8 xb[2][2];
  f32x16 acc[4][2];
};
#define DMA_ASM(M0V, VOFFV, SRD, SOFF) asm volatile("s_mov_b32 m0, %0\n\ts_nop 0\n\tbuffer_load_dwordx4 %1, %2, %3 offen lds" :: "s"(M0V), "v"(VOFFV), "s"(SRD), "s"(SOFF) : "m0", "memory")
#define DMA_ASM_NT(M0V, VOFFV, SRD, SOFF) asm volatile("s_mov_b32 m0, %0\n\ts_nop 0\n\tbuffer_load_dwordx4 %1, %2, %3 offen nt lds" :: "s"(M0V), "v"(VOFFV), "s"(SRD), "s"(SOFF) : "m0", "memory")
template <int CI, int P> DI void q_dma(QC& q) {
  if constexpr (P < 8) DMA_ASM(q.m0x + QBUF(CI) + 1024u * P, q.vxsrc[P & 1], q.srdX, q.sofx + 8192u * P + 128u * CI);
  else { constexpr int pp = P - 8; DMA_ASM(q.m0w + QBUF(CI) + 1024u * pp, q.vwsrc, q.srdW, q.sofw + 524288u * CI + 65536u * (pp >> 1) + 128u * (pp & 1)); }
}
template <int T, int P> DI void kv_dma(W4C& c) {
  if constexpr (P < 4) DMA_ASM_NT(c.m0k + tslot(T) + 1024u * P, c.vksrc[P & 1], c.srdK, c.sofk + 16384u * T + 1024u * P);
  else { constexpr int pp = P - 4; DMA_ASM_NT(c.m0v + tslot(T) + 1024u * pp, c.vvsrc, c.srdV, c.sofv + 16384u * T + 2048u * (pp >> 1) + 128u * (pp & 1)); }
}
template <int CI, int KS, int R> DI void q_read(QC& q) {
  constexpr int PAR = KS & 1;
  if constexpr (R < 8) {
    constexpr int db = R >> 1;
    if constexpr ((R & 1) == 0) asm volatile("ds_read_b64_tr_b16 %0, %1 offset:%2" : "=v"(q.alo[PAR][db]) : "v"(q.vwb + QBUF(CI)), "i"(4096 * KS + 512 * db) : "memory");
    else                        asm volatile("ds_read_b64_tr_b16 %0, %1 offset:%2" : "=v"(q.aup[PAR][db]) : "v"(q.vwb + QBUF(CI)), "i"(4096 * KS + 512 * db + 256) : "memory");
  } else {
    constexpr int qb = R - 8;
    asm volatile("ds_read_b128 %0, %1 offset:%2" : "=v"(q.xb[PAR][qb]) : "v"(q.vxb[KS] + QBUF(CI)), "i"(4096 * qb) : "memory");
  }
}
template <int CI, int KS, int M> DI void q_mfma(QC& q) {
  constexpr int PAR = KS & 1, db = M >> 1, qb = M & 1;
  const bf16x8 a = __builtin_shufflevector(q.alo[PAR][db], q.aup[PAR][db], 0, 1, 2, 3, 4, 5, 6, 7);
  if constexpr (CI == 0 && KS == 0) asm volatile("v_mfma_f32_32x32x16_bf16 %0, %1, %2, 0" : "=&v"(q.acc[db][qb]) : "v"(a), "v"(q.xb[PAR][qb]));
  else                              asm volatile("v_mfma_f32_32x32x16_bf16 %0, %1, %2, %0" : "+v"(q.acc[db][qb]) : "v"(a), "v"(q.xb[PAR][qb]));
}

constexpr int sched_dma(int ci, int ks, int m) {
  const int j = m == 1 ? 0 : (m == 4 ? 1 : (m == 7 ? 2 : -1));
  if (ci == 7 && ks == 3) return (m & 1) ? 1000 + 16 + (m >> 1) : -1;
  if (ci == 0 && ks < 3) return (m & 1) ? 200 + 4 * ks + (m >> 1) : (m == 2 ? 1000 + ks : ((m == 6 && ks == 2) ? 1003 : -1));
  if (j < 0) return -1;
  const int i = ks == 3 ? ci : ci - 1, st = ks == 3 ? 0 : ks + 1, p = 3 * st + j;
  if (i < 0) return -1;
  if (i + 3 < 8) return 100 * (i + 3) + p;
  if (i == 5) return p < 4 ? 1000 + 4 + p : -1;
  if (i == 6) return p < 8 ? 1000 + 8 + p : -1;
  return -1;
}
constexpr int sched_wo(int ci, int ks, int m) { return (ci >= 1 && ci <= 4 && (m == 2 || m == 5)) ? ((ci - 1) * 4 + ks) * 2 + (m == 5) : -1; }
constexpr int sched_vm(int ci) {
  int n = 0;
  for (int c = 0; c < 2; ++c) for (int p = 0; p < 12; ++p) { n = (c == ci + 1) ? 0 : n + 1; }
  for (int c = 0; c <= ci; ++c) for (int ks = 0; ks < 4; ++ks) { if (c == ci && ks == 3) break;
    for (int m = 0; m < 8; ++m) {
      const int d = sched_dma(c, ks, m);
      if (d >= 0) n = ((d < 1000 && d / 100 == ci + 1) || (ci == 7 && d >= 1000 && d < 1004)) ? 0 : n + 1;
      if (sched_wo(c, ks, m) >= 0) n = (ci == 7) ? 0 : n + 1;
    } }
  return n;
}
template <int I> DI void wo_load(QC& q) {
  constexpr int i = I >> 3, j = I & 7;
  asm volatile("buffer_load_ushort %0, %1, %2, %3 offen offset:%4" : "=v"(q.wo[I]) : "v"(q.vwo), "s"(q.srdO), "s"(q.sofo + 8192u * (j >> 2)), "i"(1024 * (j & 3) + 64 * i) : "memory");
}
template <int CI, int KS, int M> DI void q_gap(QC& q, W4C& c) {
  q_mfma<CI, KS, M>(q);
  constexpr int NCI = KS == 3 ? CI + 1 : CI, NKS = (KS + 1) & 3;
  if constexpr (NCI < 8 && M < 5) { q_read<NCI, NKS, 2 * M>(q); q_read<NCI, NKS, 2 * M + 1>(q); }
  constexpr int D = sched_dma(CI, KS, M), W = sched_wo(CI, KS, M);
  if constexpr (D >= 1000) kv_dma<((D - 1000) >> 3), ((D - 1000) & 7)>(c);
  else if constexpr (D >= 0) q_dma<(D / 100), (D % 100)>(q);
  if constexpr (W >= 0) wo_load<W>(q);
  if constexpr (KS == 3 && CI == 7) { k_read<2 * M>(c, tslot(0)); k_read<2 * M + 1>(c, tslot(0)); }
  GAP();
}
template <int CI, int KS, int... Ms> DI void q_step_(QC& q, W4C& c, iseq<Ms...>) {
  constexpr int PAR = KS & 1;
  if constexpr (KS == 3) {
    constexpr int VM = sched_vm(CI);
    asm volatile("s_waitcnt vmcnt(%10) lgkmcnt(0)\n\ts_barrier" : "+v"(q.alo[PAR][0]), "+v"(q.alo[PAR][1]), "+v"(q.alo[PAR][2]), "+v"(q.alo[PAR][3]),
                 "+v"(q.aup[PAR][0]), "+v"(q.aup[PAR][1]), "+v"(q.aup[PAR][2]), "+v"(q.aup[PAR][3]), "+v"(q.xb[PAR][0]), "+v"(q.xb[PAR][1]) : "i"(VM) : "memory");
  } else {
    asm volatile("s_waitcnt lgkmcnt(0)" : "+v"(q.alo[PAR][0]), "+v"(q.alo[PAR][1]), "+v"(q.alo[PAR][2]), "+v"(q.alo[PAR][3]),
                 "+v"(q.aup[PAR][0]), "+v"(q.aup[PAR][1]), "+v"(q.aup[PAR][2]), "+v"(q.aup[PAR][3]), "+v"(q.xb[PAR][0]), "+v"(q.xb[PAR][1]) :: "memory");
  }
  GAP();
  (q_gap<CI, KS, Ms>(q, c), ...);
}
template <int CI> DI void q_chunk(QC& q, W4C& c) {
  q_step_<CI, 0>(q, c, make_iseq<8>{}); q_step_<CI, 1>(q, c, make_iseq<8>{}); q_step_<CI, 2>(q, c, make_iseq<8>{}); q_step_<CI, 3>(q, c, make_iseq<8>{});
}
template <int... Cs> DI void q_chunks(QC& q, W4C& c, iseq<Cs...>) { (q_chunk<Cs>(q, c), ...); }
template <int CI, int... Ps> DI void q_dmas(QC& q, iseq<Ps...>) { (q_dma<CI, Ps>(q), ...); }
template <int... Rs> DI void q_reads0(QC& q, iseq<Rs...>) { (q_read<0, 0, Rs>(q), ...); }

template <int BASE> DI void acc_read8(float (&o)[8]) {
  asm volatile("v_accvgpr_read_b32 %0, a[%c8]\n\tv_accvgpr_read_b32 %1, a[%c9]\n\tv_accvgpr_read_b32 %2, a[%c10]\n\tv_accvgpr_read_b32 %3, a[%c11]\n\t"
               "v_accvgpr_read_b32 %4, a[%c12]\n\tv_accvgpr_read_b32 %5, a[%c13]\n\tv_accvgpr_read_b32 %6, a[%c14]\n\tv_accvgpr_read_b32 %7, a[%c15]"
               : "=v"(o[0]), "=v"(o[1]), "=v"(o[2]), "=v"(o[3]), "=v"(o[4]), "=v"(o[5]), "=v"(o[6]), "=v"(o[7])
               : "i"(BASE), "i"(BASE + 1), "i"(BASE + 2), "i"(BASE + 3), "i"(BASE + 4), "i"(BASE + 5), "i"(BASE + 6), "i"(BASE + 7));
}
template <int AQ> DI void acc_write4(const u32x4& w) {
  asm volatile("v_accvgpr_write_b32 a[%c4], %0\n\tv_accvgpr_write_b32 a[%c5], %1\n\tv_accvgpr_write_b32 a[%c6], %2\n\tv_accvgpr_write_b32 a[%c7], %3"
               :: "v"(w[0]), "v"(w[1]), "v"(w[2]), "v"(w[3]), "i"(AQ), "i"(AQ + 1), "i"(AQ + 2), "i"(AQ + 3) : W4_ACLOB_HI);
}
template <int I> DI void q_convert(QC& q, float C) {
  constexpr int qb = I & 1, d0 = I >> 1, db = d0 >> 1, s = d0 & 1;
  u32x4 w;
  #pragma unroll
  for (int e = 0; e < 4; ++e) w[e] = cvtpk(fmul_s(q.acc[db][qb][8 * s + 2 * e], C), fmul_s(q.acc[db][qb][8 * s + 2 * e + 1], C));
  acc_write4<AQ_(qb, d0)>(w);
}
template <int M> DI void gapA0(W4C& c, QC& q, float C) {
  qk_mfma<0, true, M>(c);
  GAP();
  if constexpr (M < 28 && (M & 3) < 2) { q_convert<2 * ((M >> 2) + 1) + (M & 3)>(q, C); asm volatile("s_nop 1" ::: "memory"); }
  if constexpr (M >= 24 && (M & 1) == 0) kv_dma<2, 4 + ((M - 24) >> 1)>(c);
  GAP();
}
template <int... Ms> DI void phaseA0(W4C& c, QC& q, float C, iseq<Ms...>) {
  (gapA0<Ms>(c, q, C), ...);
  asm volatile("s_waitcnt vmcnt(16) lgkmcnt(0)\n\ts_barrier" ::: "memory");
  GAP();
}
template <int I> DI void o_store(const float (&il)[2], char* obase, unsigned voff) {
  constexpr int qb = I >> 3, kk = I & 7, db = kk >> 1, s = kk & 1;
  float o[8]; acc_read8<AO_(db, qb) + 8 * s>(o);
  u32x4 w;
  #pragma unroll
  for (int e = 0; e < 4; ++e) w[e] = cvtpk(fmul_s(o[2 * e], il[qb]), fmul_s(o[2 * e + 1], il[qb]));
  __builtin_amdgcn_raw_buffer_store_b128(w, __builtin_amdgcn_make_buffer_rsrc((void*)obase, (short)0, 512 * 1024, SRD_WORD3), voff + kk * 1024, qb * (256 * 1024), ST_AUX);
}
template <int J, int KS> DI void pv_last(W4C& c) {
  constexpr int db = J >> 1, qb = J & 1, ao = AO_(db, qb);
  asm volatile("v_mfma_f32_32x32x16_bf16 a[%c2:%c3], %0, %1, a[%c2:%c3]" :: "v"(c.Vf[db][KS]), "v"(c.Pk[qb][KS]), "i"(ao), "i"(ao + 15) : W4_ACLOB);
}
template <int J> DI void drain_acc(W4C& c, const float (&il)[2], char* obase, unsigned voff) {
  constexpr int Jp = J - 1;
  pv_last<J, 0>(c); GAP();
  pv_last<J, 1>(c); GAP();
  pv_last<J, 2>(c); GAP();
  if constexpr (J > 0) { asm volatile("s_nop 7\n\ts_nop 1" ::: "memory"); o_store<8 * (Jp & 1) + 2 * (Jp >> 1)>(il, obase, voff); }
  GAP();
  pv_last<J, 3>(c); GAP();
  if constexpr (J > 0) o_store<8 * (Jp & 1) + 2 * (Jp >> 1) + 1>(il, obase, voff);
  GAP();
}
template <int... Js> DI void drain_accs(W4C& c, const float (&il)[2], char* obase, unsigned voff, iseq<Js...>) { (drain_acc<Js>(c, il, obase, voff), ...); }

constexpr size_t WS_OF = 0, WS_WOF = (size_t)16 << 20;

extern "C" __global__ __launch_bounds__(256, 1)
void attn_fused(const unsigned short* __restrict__ X, const unsigned short* __restrict__ Kc, const unsigned short* __restrict__ Vc,
                const unsigned short* __restrict__ Wq, const unsigned short* __restrict__ Wo, char* __restrict__ ws) {
  extern __shared__ __attribute__((aligned(16))) char lds[];
  const unsigned tid = threadIdx.x, lane = tid & 63u, wid = tid >> 6, l32 = lane & 31u, hi = lane >> 5;
  const unsigned g = blockIdx.x, hd = 4u * (g & 7u) + ((g >> 3) & 3u), b = g >> 5;
  const float C = 0.12751743082f;
  W4C c; QC q;
  c.ldsb = (unsigned)(size_t)lds;
  #pragma unroll
  for (int j = 0; j < 4; ++j) c.vkb[j] = c.ldsb + KOFF + 256u * l32 + 16u * ((2u * j + hi) ^ (l32 & 7u));
  c.vvb = c.ldsb + (((lane & 3u) << 3) | (((lane >> 2) & 3u) << 6) | (((lane >> 4) & 1u) << 5) | (hi << 8));
  #pragma unroll
  for (int par = 0; par < 2; ++par) c.vksrc[par] = 256u * (lane >> 4) + 16u * ((lane & 15u) ^ (4u * par + (lane >> 4)));
  c.vvsrc = 256u * ((lane >> 2) & 7u) + 64u * hi + 16u * (lane & 3u);
  c.m0k = rfl(c.ldsb + KOFF + 4096u * wid); c.m0v = rfl(c.ldsb + 4096u * wid);
  c.sofk = rfl(4096u * wid); c.sofv = rfl(4096u * wid);
  c.vvsrc2[0] = c.vvsrc + 896u; c.vvsrc2[1] = c.vvsrc; c.sofv2 = c.sofv + 32768u - 896u; c.sofk3 = c.sofk + 49152u;
  const size_t kvoff = ((size_t)b * NH + hd) * (SK * DH * 2);
  c.srdK = __builtin_amdgcn_make_buffer_rsrc((void*)((const char*)Kc + kvoff), (short)0, SK * DH * 2, SRD_WORD3);
  c.srdV = __builtin_amdgcn_make_buffer_rsrc((void*)((const char*)Vc + kvoff), (short)0, SK * DH * 2, SRD_WORD3);
  #pragma unroll
  for (int ks = 0; ks < 4; ++ks) q.vxb[ks] = c.ldsb + 8192u * wid + 128u * l32 + 16u * ((2u * ks + hi) ^ ((l32 >> 1) & 7u));
  { const unsigned p = lane & 3u, sp = ((p & 1u) << 1) | (p >> 1);
    q.vwb = c.ldsb + QWOFF + ((hi << 11) | (((lane >> 2) & 3u) << 6) | (((lane >> 4) & 1u) << 5) | (sp << 3)); }
  #pragma unroll
  for (int par = 0; par < 2; ++par) q.vxsrc[par] = 1024u * (lane >> 3) + 16u * ((lane & 7u) ^ (4u * par + (lane >> 4)));
  q.vwsrc = 8192u * ((lane >> 2) & 7u) + 64u * hi + 16u * (lane & 3u);
  q.m0x = rfl(c.ldsb + 8192u * wid); q.m0w = rfl(c.ldsb + QWOFF + 4096u * wid);
  q.sofx = rfl(65536u * wid); q.sofw = rfl(131072u * wid);
  q.srdX = __builtin_amdgcn_make_buffer_rsrc((void*)((const char*)X + (size_t)b * (SQ * DM * 2)), (short)0, SQ * DM * 2, SRD_WORD3);
  q.srdW = __builtin_amdgcn_make_buffer_rsrc((void*)((const char*)Wq + hd * 256u), (short)0, DM * NH * DH * 2 - (int)hd * 256, SRD_WORD3);
  q.srdO = __builtin_amdgcn_make_buffer_rsrc((void*)Wo, (short)0, NH * DH * DM * 2, SRD_WORD3);
  q.vwo = 4096u * hi + 2u * l32; q.sofo = rfl(16384u * g + 256u * wid);
  asm volatile("s_nop 4" ::: "memory");
  q_dmas<0>(q, make_iseq<12>{});
  q_dmas<1>(q, make_iseq<12>{});
  asm volatile("s_waitcnt vmcnt(12)\n\ts_barrier" ::: "memory");
  q_reads0(q, make_iseq<10>{});
  q_chunks(q, c, make_iseq<8>{});
  asm volatile("" : "+v"(q.wo[0]), "+v"(q.wo[1]), "+v"(q.wo[2]), "+v"(q.wo[3]), "+v"(q.wo[4]), "+v"(q.wo[5]), "+v"(q.wo[6]), "+v"(q.wo[7]),
                    "+v"(q.wo[8]), "+v"(q.wo[9]), "+v"(q.wo[10]), "+v"(q.wo[11]), "+v"(q.wo[12]), "+v"(q.wo[13]), "+v"(q.wo[14]), "+v"(q.wo[15]));
  asm volatile("" : "+v"(q.wo[16]), "+v"(q.wo[17]), "+v"(q.wo[18]), "+v"(q.wo[19]), "+v"(q.wo[20]), "+v"(q.wo[21]), "+v"(q.wo[22]), "+v"(q.wo[23]),
                    "+v"(q.wo[24]), "+v"(q.wo[25]), "+v"(q.wo[26]), "+v"(q.wo[27]), "+v"(q.wo[28]), "+v"(q.wo[29]), "+v"(q.wo[30]), "+v"(q.wo[31]));
  #pragma unroll
  for (int i = 0; i < 4; ++i) {
    u32x4 w;
    #pragma unroll
    for (int e = 0; e < 4; ++e) w[e] = q.wo[8 * i + 2 * e] | (q.wo[8 * i + 2 * e + 1] << 16);
    __builtin_amdgcn_raw_buffer_store_b128(w, __builtin_amdgcn_make_buffer_rsrc((void*)(ws + WS_WOF), (short)0, 4 << 20, SRD_WORD3), lane * 16u, rfl(((4u * wid + i) * 256u + g) * 1024u), ST_AUX);
  }
  asm volatile("s_nop 7\n\ts_nop 7" : "+v"(q.acc[0][0]), "+v"(q.acc[0][1]), "+v"(q.acc[1][0]), "+v"(q.acc[1][1]), "+v"(q.acc[2][0]), "+v"(q.acc[2][1]), "+v"(q.acc[3][0]), "+v"(q.acc[3][1]) :: "memory");
  q_convert<0>(q, C); q_convert<1>(q, C);
  asm volatile("s_waitcnt lgkmcnt(0)\n\ts_nop 1" ::: "memory");
  c.mhat[0] = c.mhat[1] = 0.f; c.ls[0][0] = c.ls[0][1] = c.ls[1][0] = c.ls[1][1] = 0.f; c.fct[0] = c.fct[1] = 1.f; c.pend = false;
  phaseA0(c, q, C, make_iseq<32>{});                                   phaseB<0, true,  false, true, true>(c, tslot(1), tslot(3), 0u);
  phaseA<1, true, 16>(c, tslot(0), tslot(3), 16384u);                  phaseB<1, false, true,  true, true>(c, tslot(2), tslot(4), 16384u);
  {
    unsigned s0 = tslot(1), s1 = tslot(2), s2 = tslot(3), s3 = tslot(4), s4 = tslot(5), tb = 32768u;
    for (int t = 2; t < 12; t += 2) {
      phaseA<0, true, 12>(c, s0, s3, tb);          phaseB<0, false, false, true, true>(c, s2, s4, tb);
      phaseA<1, true, 12>(c, s1, s4, tb + 16384u); phaseB<1, false, false, true, true>(c, s3, s0, tb + 16384u);
      const unsigned n0 = s2, n1 = s3, n2 = s4, n3 = s0, n4 = s1; s0 = n0; s1 = n1; s2 = n2; s3 = n3; s4 = n4; tb += 32768u;
    }
  }
  phaseA<0, true, 12>(c, tslot(11), tslot(14), 12u * 16384u);  phaseB<0, false, false, true,  true>(c, tslot(13), tslot(15), 12u * 16384u);
  phaseA<1, true, 12>(c, tslot(12), tslot(15), 13u * 16384u);  phaseB<1, false, false, false, true>(c, tslot(14), 0u, 0u);
  phaseA<0, false, 4>(c, tslot(13), 0u, 0u);                   phaseB<0, false, false, false, true>(c, tslot(15), 0u, 0u);
  phaseA<1, false, 0>(c, tslot(14), 0u, 0u);                   phaseB<1, false, false, false, false>(c, 0u, 0u, 0u);
  {
    const unsigned vslot = tslot(NT - 1);
    tr_reads(c, vslot, make_iseq<32>{});
    exps_from40<1>(c, make_iseq<24>{});
    cvts<1>(c, make_iseq<32>{});
    adds_from18<1>(c, make_iseq<46>{});
    float lsum[2];
    #pragma unroll
    for (int qb = 0; qb < 2; ++qb) { const float rs = fadd_s(c.ls[qb][0], c.ls[qb][1]);
      auto pr = __builtin_amdgcn_permlane32_swap(__float_as_uint(rs), __float_as_uint(rs), false, false);
      lsum[qb] = fadd_s(__uint_as_float(pr[0]), __uint_as_float(pr[1])); }
    asm volatile("s_waitcnt lgkmcnt(0)" : W4_VLO :: "memory"); asm volatile("" : W4_VUP :: "memory");
    GAP();
    #pragma unroll
    for (int ks = 0; ks < 4; ++ks)
      #pragma unroll
      for (int db = 0; db < 4; ++db) c.Vf[db][ks] = __builtin_shufflevector(c.vlo[db][ks], c.vup[db][ks], 0, 1, 2, 3, 4, 5, 6, 7);
    asm volatile("s_nop 1" ::: "memory");
    float il[2];
    il[0] = __builtin_amdgcn_rcpf(lsum[0]); il[1] = __builtin_amdgcn_rcpf(lsum[1]);
    asm volatile("s_nop 1" : "+v"(il[0]), "+v"(il[1]));
    char* obase = ws + WS_OF + (size_t)rfl(((b * 8u + 2u * wid) * 256u + hd * 8u) * 1024u);
    drain_accs(c, il, obase, lane * 16u, make_iseq<8>{});
    asm volatile("s_nop 7\n\ts_nop 7" ::: "memory");
    o_store<8 + 6>(il, obase, lane * 16u); o_store<8 + 7>(il, obase, lane * 16u);
  }
}

extern "C" __global__ __launch_bounds__(512, 2)
void oproj(const char* __restrict__ ws, unsigned short* __restrict__ out) {
  __shared__ __attribute__((aligned(16))) float red[8 * 64 * 68];
  const unsigned tid = threadIdx.x, lane = tid & 63u, wid = tid >> 6, l32 = lane & 31u, hi = lane >> 5;
  const unsigned g = blockIdx.x, xc = g & 7u, idx = g >> 3;
  const unsigned qt = 4u * xc + (idx & 3u), nt = idx >> 2;
  const char* ofp = ws + WS_OF + ((size_t)(2u * qt) * 256 + 32u * wid) * 1024 + lane * 16u;
  const char* wfp = ws + WS_WOF + ((size_t)(2u * nt) * 256 + 32u * wid) * 1024 + lane * 16u;
  f32x16 acc[2][2] = {};
  bf16x8 fo[4][2], fw[4][2];
  #pragma unroll
  for (int i = 0; i < 4; ++i) {
    fo[i][0] = *(const bf16x8*)(ofp + i * 1024); fo[i][1] = *(const bf16x8*)(ofp + 262144 + i * 1024);
    fw[i][0] = *(const bf16x8*)(wfp + i * 1024); fw[i][1] = *(const bf16x8*)(wfp + 262144 + i * 1024);
  }
  #pragma unroll 1
  for (int k0 = 0; k0 < 32; k0 += 4) {
    #pragma unroll
    for (int i = 0; i < 4; ++i) {
      const bf16x8 o0 = fo[i][0], o1 = fo[i][1], w0 = fw[i][0], w1 = fw[i][1];
      if (k0 + 4 < 32) {
        const int kn = k0 + 4 + i;
        fo[i][0] = *(const bf16x8*)(ofp + kn * 1024); fo[i][1] = *(const bf16x8*)(ofp + 262144 + kn * 1024);
        fw[i][0] = *(const bf16x8*)(wfp + kn * 1024); fw[i][1] = *(const bf16x8*)(wfp + 262144 + kn * 1024);
      }
      acc[0][0] = __builtin_amdgcn_mfma_f32_32x32x16_bf16(w0, o0, acc[0][0], 0, 0, 0);
      acc[0][1] = __builtin_amdgcn_mfma_f32_32x32x16_bf16(w0, o1, acc[0][1], 0, 0, 0);
      acc[1][0] = __builtin_amdgcn_mfma_f32_32x32x16_bf16(w1, o0, acc[1][0], 0, 0, 0);
      acc[1][1] = __builtin_amdgcn_mfma_f32_32x32x16_bf16(w1, o1, acc[1][1], 0, 0, 0);
    }
  }
  float* rw = red + wid * (64 * 68);
  #pragma unroll
  for (int nb = 0; nb < 2; ++nb)
    #pragma unroll
    for (int qb = 0; qb < 2; ++qb)
      #pragma unroll
      for (int gq = 0; gq < 4; ++gq) {
        f32x4 v = {acc[nb][qb][4 * gq], acc[nb][qb][4 * gq + 1], acc[nb][qb][4 * gq + 2], acc[nb][qb][4 * gq + 3]};
        *(f32x4*)(rw + (32 * qb + l32) * 68 + 32 * nb + 8 * gq + 4 * hi) = v;
      }
  __syncthreads();
  {
    const unsigned qr = tid >> 3, n0 = (tid & 7u) * 8u;
    f32x4 s0 = {}, s1 = {};
    #pragma unroll
    for (int w = 0; w < 8; ++w) {
      s0 += *(const f32x4*)(red + w * (64 * 68) + qr * 68 + n0);
      s1 += *(const f32x4*)(red + w * (64 * 68) + qr * 68 + n0 + 4);
    }
    u32x4 w; w[0] = cvtpk(s0[0], s0[1]); w[1] = cvtpk(s0[2], s0[3]); w[2] = cvtpk(s1[0], s1[1]); w[3] = cvtpk(s1[2], s1[3]);
    *(u32x4*)(out + (size_t)(64u * qt + qr) * DM + 64u * nt + n0) = w;
  }
}

extern "C" void kernel_launch(void* const* d_in, const int* in_sizes, int n_in, void* d_out, int out_size, void* d_ws, size_t ws_size, hipStream_t stream) {
  (void)in_sizes; (void)n_in; (void)out_size; (void)ws_size;
  (void)hipFuncSetAttribute((const void*)attn_fused, hipFuncAttributeMaxDynamicSharedMemorySize, (int)SHM_ATTN);
  hipLaunchKernelGGL(attn_fused, dim3(256), dim3(256), SHM_ATTN, stream, (const unsigned short*)d_in[0], (const unsigned short*)d_in[1], (const unsigned short*)d_in[2],
                     (const unsigned short*)d_in[3], (const unsigned short*)d_in[4], (char*)d_ws);
  hipLaunchKernelGGL(oproj, dim3(256), dim3(512), 0, stream, (const char*)d_ws, (unsigned short*)d_out);
}
```

```cpp
#include <hip/hip_runtime.h>
#include <utility>

constexpr int NB = 8, SQ = 256, DM = 512, NH = 32, SK = 1024, DH = 128, NT = SK / 64;
constexpr float THR = 8.0f;
constexpr size_t SHM_ATTN = 163840;
typedef float    f32x16 __attribute__((ext_vector_type(16)));
typedef float    f32x2  __attribute__((ext_vector_type(2)));
typedef float    f32x4  __attribute__((ext_vector_type(4)));
typedef __bf16   bf16x8 __attribute__((ext_vector_type(8)));
typedef __bf16   bf16x4 __attribute__((ext_vector_type(4)));
typedef __bf16   bf16x2 __attribute__((ext_vector_type(2)));
typedef unsigned u32x4  __attribute__((ext_vector_type(4)));
typedef unsigned u32x2  __attribute__((ext_vector_type(2)));
template <int... Is> using iseq = std::integer_sequence<int, Is...>;
template <int N> using make_iseq = std::make_integer_sequence<int, N>;

#define SLOTB 0x8000u
#define KOFF  0x4000u
#define QBUF(i) (49152u * ((i) % 3))
__device__ __forceinline__ constexpr unsigned tslot(unsigned t) { return ((t + 4u) % 5u) * SLOTB; }
#define QWOFF 32768u
#define SRD_WORD3 0x00020000
#define ST_AUX 16
#define DI __device__ __forceinline__
#define GAP() __builtin_amdgcn_sched_barrier(0)
#define W4_ACLOB "a0","a1","a2","a3","a4","a5","a6","a7","a8","a9","a10","a11","a12","a13","a14","a15","a16","a17","a18","a19","a20","a21","a22","a23","a24","a25","a26","a27","a28","a29","a30","a31","a32","a33","a34","a35","a36","a37","a38","a39","a40","a41","a42","a43","a44","a45","a46","a47","a48","a49","a50","a51","a52","a53","a54","a55","a56","a57","a58","a59","a60","a61","a62","a63","a64","a65","a66","a67","a68","a69","a70","a71","a72","a73","a74","a75","a76","a77","a78","a79","a80","a81","a82","a83","a84","a85","a86","a87","a88","a89","a90","a91","a92","a93","a94","a95","a96","a97","a98","a99","a100","a101","a102","a103","a104","a105","a106","a107","a108","a109","a110","a111","a112","a113","a114","a115","a116","a117","a118","a119","a120","a121","a122","a123","a124","a125","a126","a127"
#define W4_ACLOB_HI "a128","a129","a130","a131","a132","a133","a134","a135","a136","a137","a138","a139","a140","a141","a142","a143","a144","a145","a146","a147","a148","a149","a150","a151","a152","a153","a154","a155","a156","a157","a158","a159","a160","a161","a162","a163","a164","a165","a166","a167","a168","a169","a170","a171","a172","a173","a174","a175","a176","a177","a178","a179","a180","a181","a182","a183","a184","a185","a186","a187","a188","a189","a190","a191","a192","a193","a194","a195","a196","a197","a198","a199","a200","a201","a202","a203","a204","a205","a206","a207","a208","a209","a210","a211","a212","a213","a214","a215","a216","a217","a218","a219","a220","a221","a222","a223","a224","a225","a226","a227","a228","a229","a230","a231","a232","a233","a234","a235","a236","a237","a238","a239","a240","a241","a242","a243","a244","a245","a246","a247","a248","a249","a250","a251","a252","a253","a254","a255"
#define W4_RESCALE_TEXT "v_accvgpr_read_b32 %0, a0\n\tv_mul_f32 %0, %2, %0\n\tv_accvgpr_write_b32 a0, %0\n\t" "v_accvgpr_read_b32 %1, a1\n\tv_mul_f32 %1, %2, %1\n\tv_accvgpr_write_b32 a1, %1\n\t" "v_accvgpr_read_b32 %0, a2\n\tv_mul_f32 %0, %2, %0\n\tv_accvgpr_write_b32 a2, %0\n\t" "v_accvgpr_read_b32 %1, a3\n\tv_mul_f32 %1, %2, %1\n\tv_accvgpr_write_b32 a3, %1\n\t" "v_accvgpr_read_b32 %0, a4\n\tv_mul_f32 %0, %2, %0\n\tv_accvgpr_write_b32 a4, %0\n\t" "v_accvgpr_read_b32 %1, a5\n\tv_mul_f32 %1, %2, %1\n\tv_accvgpr_write_b32 a5, %1\n\t" "v_accvgpr_read_b32 %0, a6\n\tv_mul_f32 %0, %2, %0\n\tv_accvgpr_write_b32 a6, %0\n\t" "v_accvgpr_read_b32 %1, a7\n\tv_mul_f32 %1, %2, %1\n\tv_accvgpr_write_b32 a7, %1\n\t" "v_accvgpr_read_b32 %0, a8\n\tv_mul_f32 %0, %2, %0\n\tv_accvgpr_write_b32 a8, %0\n\t" "v_accvgpr_read_b32 %1, a9\n\tv_mul_f32 %1, %2, %1\n\tv_accvgpr_write_b32 a9, %1\n\t" "v_accvgpr_read_b32 %0, a10\n\tv_mul_f32 %0, %2, %0\n\tv_accvgpr_write_b32 a10, %0\n\t" "v_accvgpr_read_b32 %1, a11\n\tv_mul_f32 %1, %2, %1\n\tv_accvgpr_write_b32 a11, %1\n\t" "v_accvgpr_read_b32 %0, a12\n\tv_mul_f32 %0, %2, %0\n\tv_accvgpr_write_b32 a12, %0\n\t" "v_accvgpr_read_b32 %1, a13\n\tv_mul_f32 %1, %2, %1\n\tv_accvgpr_write_b32 a13, %1\n\t" "v_accvgpr_read_b32 %0, a14\n\tv_mul_f32 %0, %2, %0\n\tv_accvgpr_write_b32 a14, %0\n\t" "v_accvgpr_read_b32 %1, a15\n\tv_mul_f32 %1, %2, %1\n\tv_accvgpr_write_b32 a15, %1\n\t" "v_accvgpr_read_b32 %0, a16\n\tv_mul_f32 %0, %3, %0\n\tv_accvgpr_write_b32 a16, %0\n\t" "v_accvgpr_read_b32 %1, a17\n\tv_mul_f32 %1, %3, %1\n\tv_accvgpr_write_b32 a17, %1\n\t" "v_accvgpr_read_b32 %0, a18\n\tv_mul_f32 %0, %3, %0\n\tv_accvgpr_write_b32 a18, %0\n\t" "v_accvgpr_read_b32 %1, a19\n\tv_mul_f32 %1, %3, %1\n\tv_accvgpr_write_b32 a19, %1\n\t" "v_accvgpr_read_b32 %0, a20\n\tv_mul_f32 %0, %3, %0\n\tv_accvgpr_write_b32 a20, %0\n\t" "v_accvgpr_read_b32 %1, a21\n\tv_mul_f32 %1, %3, %1\n\tv_accvgpr_write_b32 a21, %1\n\t" "v_accvgpr_read_b32 %0, a22\n\tv_mul_f32 %0, %3, %0\n\tv_accvgpr_write_b32 a22, %0\n\t" "v_accvgpr_read_b32 %1, a23\n\tv_mul_f32 %1, %3, %1\n\tv_accvgpr_write_b32 a23, %1\n\t" "v_accvgpr_read_b32 %0, a24\n\tv_mul_f32 %0, %3, %0\n\tv_accvgpr_write_b32 a24, %0\n\t" "v_accvgpr_read_b32 %1, a25\n\tv_mul_f32 %1, %3, %1\n\tv_accvgpr_write_b32 a25, %1\n\t" "v_accvgpr_read_b32 %0, a26\n\tv_mul_f32 %0, %3, %0\n\tv_accvgpr_write_b32 a26, %0\n\t" "v_accvgpr_read_b32 %1, a27\n\tv_mul_f32 %1, %3, %1\n\tv_accvgpr_write_b32 a27, %1\n\t" "v_accvgpr_read_b32 %0, a28\n\tv_mul_f32 %0, %3, %0\n\tv_accvgpr_write_b32 a28, %0\n\t" "v_accvgpr_read_b32 %1, a29\n\tv_mul_f32 %1, %3, %1\n\tv_accvgpr_write_b32 a29, %1\n\t" "v_accvgpr_read_b32 %0, a30\n\tv_mul_f32 %0, %3, %0\n\tv_accvgpr_write_b32 a30, %0\n\t" "v_accvgpr_read_b32 %1, a31\n\tv_mul_f32 %1, %3, %1\n\tv_accvgpr_write_b32 a31, %1\n\t" "v_accvgpr_read_b32 %0, a32\n\tv_mul_f32 %0, %2, %0\n\tv_accvgpr_write_b32 a32, %0\n\t" "v_accvgpr_read_b32 %1, a33\n\tv_mul_f32 %1, %2, %1\n\tv_accvgpr_write_b32 a33, %1\n\t" "v_accvgpr_read_b32 %0, a34\n\tv_mul_f32 %0, %2, %0\n\tv_accvgpr_write_b32 a34, %0\n\t" "v_accvgpr_read_b32 %1, a35\n\tv_mul_f32 %1, %2, %1\n\tv_accvgpr_write_b32 a35, %1\n\t" "v_accvgpr_read_b32 %0, a36\n\tv_mul_f32 %0, %2, %0\n\tv_accvgpr_write_b32 a36, %0\n\t" "v_accvgpr_read_b32 %1, a37\n\tv_mul_f32 %1, %2, %1\n\tv_accvgpr_write_b32 a37, %1\n\t" "v_accvgpr_read_b32 %0, a38\n\tv_mul_f32 %0, %2, %0\n\tv_accvgpr_write_b32 a38, %0\n\t" "v_accvgpr_read_b32 %1, a39\n\tv_mul_f32 %1, %2, %1\n\tv_accvgpr_write_b32 a39, %1\n\t" "v_accvgpr_read_b32 %0, a40\n\tv_mul_f32 %0, %2, %0\n\tv_accvgpr_write_b32 a40, %0\n\t" "v_accvgpr_read_b32 %1, a41\n\tv_mul_f32 %1, %2, %1\n\tv_accvgpr_write_b32 a41, %1\n\t" "v_accvgpr_read_b32 %0, a42\n\tv_mul_f32 %0, %2, %0\n\tv_accvgpr_write_b32 a42, %0\n\t" "v_accvgpr_read_b32 %1, a43\n\tv_mul_f32 %1, %2, %1\n\tv_accvgpr_write_b32 a43, %1\n\t" "v_accvgpr_read_b32 %0, a44\n\tv_mul_f32 %0, %2, %0\n\tv_accvgpr_write_b32 a44, %0\n\t" "v_accvgpr_read_b32 %1, a45\n\tv_mul_f32 %1, %2, %1\n\tv_accvgpr_write_b32 a45, %1\n\t" "v_accvgpr_read_b32 %0, a46\n\tv_mul_f32 %0, %2, %0\n\tv_accvgpr_write_b32 a46, %0\n\t" "v_accvgpr_read_b32 %1, a47\n\tv_mul_f32 %1, %2, %1\n\tv_accvgpr_write_b32 a47, %1\n\t" "v_accvgpr_read_b32 %0, a48\n\tv_mul_f32 %0, %3, %0\n\tv_accvgpr_write_b32 a48, %0\n\t" "v_accvgpr_read_b32 %1, a49\n\tv_mul_f32 %1, %3, %1\n\tv_accvgpr_write_b32 a49, %1\n\t" "v_accvgpr_read_b32 %0, a50\n\tv_mul_f32 %0, %3, %0\n\tv_accvgpr_write_b32 a50, %0\n\t" "v_accvgpr_read_b32 %1, a51\n\tv_mul_f32 %1, %3, %1\n\tv_accvgpr_write_b32 a51, %1\n\t" "v_accvgpr_read_b32 %0, a52\n\tv_mul_f32 %0, %3, %0\n\tv_accvgpr_write_b32 a52, %0\n\t" "v_accvgpr_read_b32 %1, a53\n\tv_mul_f32 %1, %3, %1\n\tv_accvgpr_write_b32 a53, %1\n\t" "v_accvgpr_read_b32 %0, a54\n\tv_mul_f32 %0, %3, %0\n\tv_accvgpr_write_b32 a54, %0\n\t" "v_accvgpr_read_b32 %1, a55\n\tv_mul_f32 %1, %3, %1\n\tv_accvgpr_write_b32 a55, %1\n\t" "v_accvgpr_read_b32 %0, a56\n\tv_mul_f32 %0, %3, %0\n\tv_accvgpr_write_b32 a56, %0\n\t" "v_accvgpr_read_b32 %1, a57\n\tv_mul_f32 %1, %3, %1\n\tv_accvgpr_write_b32 a57, %1\n\t" "v_accvgpr_read_b32 %0, a58\n\tv_mul_f32 %0, %3, %0\n\tv_accvgpr_write_b32 a58, %0\n\t" "v_accvgpr_read_b32 %1, a59\n\tv_mul_f32 %1, %3, %1\n\tv_accvgpr_write_b32 a59, %1\n\t" "v_accvgpr_read_b32 %0, a60\n\tv_mul_f32 %0, %3, %0\n\tv_accvgpr_write_b32 a60, %0\n\t" "v_accvgpr_read_b32 %1, a61\n\tv_mul_f32 %1, %3, %1\n\tv_accvgpr_write_b32 a61, %1\n\t" "v_accvgpr_read_b32 %0, a62\n\tv_mul_f32 %0, %3, %0\n\tv_accvgpr_write_b32 a62, %0\n\t" "v_accvgpr_read_b32 %1, a63\n\tv_mul_f32 %1, %3, %1\n\tv_accvgpr_write_b32 a63, %1\n\t" "v_accvgpr_read_b32 %0, a64\n\tv_mul_f32 %0, %2, %0\n\tv_accvgpr_write_b32 a64, %0\n\t" "v_accvgpr_read_b32 %1, a65\n\tv_mul_f32 %1, %2, %1\n\tv_accvgpr_write_b32 a65, %1\n\t" "v_accvgpr_read_b32 %0, a66\n\tv_mul_f32 %0, %2, %0\n\tv_accvgpr_write_b32 a66, %0\n\t" "v_accvgpr_read_b32 %1, a67\n\tv_mul_f32 %1, %2, %1\n\tv_accvgpr_write_b32 a67, %1\n\t" "v_accvgpr_read_b32 %0, a68\n\tv_mul_f32 %0, %2, %0\n\tv_accvgpr_write_b32 a68, %0\n\t" "v_accvgpr_read_b32 %1, a69\n\tv_mul_f32 %1, %2, %1\n\tv_accvgpr_write_b32 a69, %1\n\t" "v_accvgpr_read_b32 %0, a70\n\tv_mul_f32 %0, %2, %0\n\tv_accvgpr_write_b32 a70, %0\n\t" "v_accvgpr_read_b32 %1, a71\n\tv_mul_f32 %1, %2, %1\n\tv_accvgpr_write_b32 a71, %1\n\t" "v_accvgpr_read_b32 %0, a72\n\tv_mul_f32 %0, %2, %0\n\tv_accvgpr_write_b32 a72, %0\n\t" "v_accvgpr_read_b32 %1, a73\n\tv_mul_f32 %1, %2, %1\n\tv_accvgpr_write_b32 a73, %1\n\t" "v_accvgpr_read_b32 %0, a74\n\tv_mul_f32 %0, %2, %0\n\tv_accvgpr_write_b32 a74, %0\n\t" "v_accvgpr_read_b32 %1, a75\n\tv_mul_f32 %1, %2, %1\n\tv_accvgpr_write_b32 a75, %1\n\t" "v_accvgpr_read_b32 %0, a76\n\tv_mul_f32 %0, %2, %0\n\tv_accvgpr_write_b32 a76, %0\n\t" "v_accvgpr_read_b32 %1, a77\n\tv_mul_f32 %1, %2, %1\n\tv_accvgpr_write_b32 a77, %1\n\t" "v_accvgpr_read_b32 %0, a78\n\tv_mul_f32 %0, %2, %0\n\tv_accvgpr_write_b32 a78, %0\n\t" "v_accvgpr_read_b32 %1, a79\n\tv_mul_f32 %1, %2, %1\n\tv_accvgpr_write_b32 a79, %1\n\t" "v_accvgpr_read_b32 %0, a80\n\tv_mul_f32 %0, %3, %0\n\tv_accvgpr_write_b32 a80, %0\n\t" "v_accvgpr_read_b32 %1, a81\n\tv_mul_f32 %1, %3, %1\n\tv_accvgpr_write_b32 a81, %1\n\t" "v_accvgpr_read_b32 %0, a82\n\tv_mul_f32 %0, %3, %0\n\tv_accvgpr_write_b32 a82, %0\n\t" "v_accvgpr_read_b32 %1, a83\n\tv_mul_f32 %1, %3, %1\n\tv_accvgpr_write_b32 a83, %1\n\t" "v_accvgpr_read_b32 %0, a84\n\tv_mul_f32 %0, %3, %0\n\tv_accvgpr_write_b32 a84, %0\n\t" "v_accvgpr_read_b32 %1, a85\n\tv_mul_f32 %1, %3, %1\n\tv_accvgpr_write_b32 a85, %1\n\t" "v_accvgpr_read_b32 %0, a86\n\tv_mul_f32 %0, %3, %0\n\tv_accvgpr_write_b32 a86, %0\n\t" "v_accvgpr_read_b32 %1, a87\n\tv_mul_f32 %1, %3, %1\n\tv_accvgpr_write_b32 a87, %1\n\t" "v_accvgpr_read_b32 %0, a88\n\tv_mul_f32 %0, %3, %0\n\tv_accvgpr_write_b32 a88, %0\n\t" "v_accvgpr_read_b32 %1, a89\n\tv_mul_f32 %1, %3, %1\n\tv_accvgpr_write_b32 a89, %1\n\t" "v_accvgpr_read_b32 %0, a90\n\tv_mul_f32 %0, %3, %0\n\tv_accvgpr_write_b32 a90, %0\n\t" "v_accvgpr_read_b32 %1, a91\n\tv_mul_f32 %1, %3, %1\n\tv_accvgpr_write_b32 a91, %1\n\t" "v_accvgpr_read_b32 %0, a92\n\tv_mul_f32 %0, %3, %0\n\tv_accvgpr_write_b32 a92, %0\n\t" "v_accvgpr_read_b32 %1, a93\n\tv_mul_f32 %1, %3, %1\n\tv_accvgpr_write_b32 a93, %1\n\t" "v_accvgpr_read_b32 %0, a94\n\tv_mul_f32 %0, %3, %0\n\tv_accvgpr_write_b32 a94, %0\n\t" "v_accvgpr_read_b32 %1, a95\n\tv_mul_f32 %1, %3, %1\n\tv_accvgpr_write_b32 a95, %1\n\t" "v_accvgpr_read_b32 %0, a96\n\tv_mul_f32 %0, %2, %0\n\tv_accvgpr_write_b32 a96, %0\n\t" "v_accvgpr_read_b32 %1, a97\n\tv_mul_f32 %1, %2, %1\n\tv_accvgpr_write_b32 a97, %1\n\t" "v_accvgpr_read_b32 %0, a98\n\tv_mul_f32 %0, %2, %0\n\tv_accvgpr_write_b32 a98, %0\n\t" "v_accvgpr_read_b32 %1, a99\n\tv_mul_f32 %1, %2, %1\n\tv_accvgpr_write_b32 a99, %1\n\t" "v_accvgpr_read_b32 %0, a100\n\tv_mul_f32 %0, %2, %0\n\tv_accvgpr_write_b32 a100, %0\n\t" "v_accvgpr_read_b32 %1, a101\n\tv_mul_f32 %1, %2, %1\n\tv_accvgpr_write_b32 a101, %1\n\t" "v_accvgpr_read_b32 %0, a102\n\tv_mul_f32 %0, %2, %0\n\tv_accvgpr_write_b32 a102, %0\n\t" "v_accvgpr_read_b32 %1, a103\n\tv_mul_f32 %1, %2, %1\n\tv_accvgpr_write_b32 a103, %1\n\t" "v_accvgpr_read_b32 %0, a104\n\tv_mul_f32 %0, %2, %0\n\tv_accvgpr_write_b32 a104, %0\n\t" "v_accvgpr_read_b32 %1, a105\n\tv_mul_f32 %1, %2, %1\n\tv_accvgpr_write_b32 a105, %1\n\t" "v_accvgpr_read_b32 %0, a106\n\tv_mul_f32 %0, %2, %0\n\tv_accvgpr_write_b32 a106, %0\n\t" "v_accvgpr_read_b32 %1, a107\n\tv_mul_f32 %1, %2, %1\n\tv_accvgpr_write_b32 a107, %1\n\t" "v_accvgpr_read_b32 %0, a108\n\tv_mul_f32 %0, %2, %0\n\tv_accvgpr_write_b32 a108, %0\n\t" "v_accvgpr_read_b32 %1, a109\n\tv_mul_f32 %1, %2, %1\n\tv_accvgpr_write_b32 a109, %1\n\t" "v_accvgpr_read_b32 %0, a110\n\tv_mul_f32 %0, %2, %0\n\tv_accvgpr_write_b32 a110, %0\n\t" "v_accvgpr_read_b32 %1, a111\n\tv_mul_f32 %1, %2, %1\n\tv_accvgpr_write_b32 a111, %1\n\t" "v_accvgpr_read_b32 %0, a112\n\tv_mul_f32 %0, %3, %0\n\tv_accvgpr_write_b32 a112, %0\n\t" "v_accvgpr_read_b32 %1, a113\n\tv_mul_f32 %1, %3, %1\n\tv_accvgpr_write_b32 a113, %1\n\t" "v_accvgpr_read_b32 %0, a114\n\tv_mul_f32 %0, %3, %0\n\tv_accvgpr_write_b32 a114, %0\n\t" "v_accvgpr_read_b32 %1, a115\n\tv_mul_f32 %1, %3, %1\n\tv_accvgpr_write_b32 a115, %1\n\t" "v_accvgpr_read_b32 %0, a116\n\tv_mul_f32 %0, %3, %0\n\tv_accvgpr_write_b32 a116, %0\n\t" "v_accvgpr_read_b32 %1, a117\n\tv_mul_f32 %1, %3, %1\n\tv_accvgpr_write_b32 a117, %1\n\t" "v_accvgpr_read_b32 %0, a118\n\tv_mul_f32 %0, %3, %0\n\tv_accvgpr_write_b32 a118, %0\n\t" "v_accvgpr_read_b32 %1, a119\n\tv_mul_f32 %1, %3, %1\n\tv_accvgpr_write_b32 a119, %1\n\t" "v_accvgpr_read_b32 %0, a120\n\tv_mul_f32 %0, %3, %0\n\tv_accvgpr_write_b32 a120, %0\n\t" "v_accvgpr_read_b32 %1, a121\n\tv_mul_f32 %1, %3, %1\n\tv_accvgpr_write_b32 a121, %1\n\t" "v_accvgpr_read_b32 %0, a122\n\tv_mul_f32 %0, %3, %0\n\tv_accvgpr_write_b32 a122, %0\n\t" "v_accvgpr_read_b32 %1, a123\n\tv_mul_f32 %1, %3, %1\n\tv_accvgpr_write_b32 a123, %1\n\t" "v_accvgpr_read_b32 %0, a124\n\tv_mul_f32 %0, %3, %0\n\tv_accvgpr_write_b32 a124, %0\n\t" "v_accvgpr_read_b32 %1, a125\n\tv_mul_f32 %1, %3, %1\n\tv_accvgpr_write_b32 a125, %1\n\t" "v_accvgpr_read_b32 %0, a126\n\tv_mul_f32 %0, %3, %0\n\tv_accvgpr_write_b32 a126, %0\n\t" "v_accvgpr_read_b32 %1, a127\n\tv_mul_f32 %1, %3, %1\n\tv_accvgpr_write_b32 a127, %1\n\t"


DI unsigned rfl(unsigned x) { return __builtin_amdgcn_readfirstlane(x); }
DI unsigned cvtpk(float lo, float hi) {
  f32x2 v = {lo, hi}; bf16x2 b = __builtin_convertvector(v, bf16x2); return __builtin_bit_cast(unsigned, b);
}
DI float max3f(float a, float b, float c) { float r; asm("v_max3_f32 %0, %1, %2, %3" : "=v"(r) : "v"(a), "v"(b), "v"(c)); return r; }
DI float max2f(float a, float b) { float r; asm("v_max_f32_e32 %0, %1, %2" : "=v"(r) : "v"(a), "v"(b)); return r; }
DI float fadd_s(float a, float b) { float r; asm("v_add_f32_e32 %0, %1, %2" : "=v"(r) : "v"(a), "v"(b)); return r; }
DI float fmul_s(float a, float b) { float r; asm("v_mul_f32_e32 %0, %1, %2" : "=v"(r) : "v"(a), "v"(b)); return r; }
DI float fsub_s(float a, float b) { float r; asm("v_sub_f32_e32 %0, %1, %2" : "=v"(r) : "v"(a), "v"(b)); return r; }
DI float w4_aone() { unsigned l; asm volatile("v_mbcnt_lo_u32_b32 %0, -1, 0\n\tv_mbcnt_hi_u32_b32 %0, -1, %0" : "=v"(l)); return l < 32u ? 1.f : 0.f; }
DI void negm_bcast(f32x16& negm, float aone, float neg_mhat) {
  asm volatile("s_nop 1\n\tv_mfma_f32_32x32x2_f32 %0, %1, %2, 0" : "+v"(negm) : "v"(aone), "v"(neg_mhat));
}

struct W4C {
  unsigned vkb[4], vvb, vksrc[2], vvsrc, m0k, m0v, sofk, sofv, ldsb;
  __amdgpu_buffer_rsrc_t srdK, srdV;
  f32x16 S[2][2][2];
  f32x16 NEGM[2];
  bf16x8 Vf[4][4];
  bf16x8 Pk[2][4];
  bf16x4 vlo[4][4], vup[4][4];
  float mhat[2], ls[2][2], fct[2]; bool pend;
  float pmx0[2], pmx1[2], rmax[2];
  unsigned vvsrc2[2], sofv2, sofk3;
};

constexpr int AO_(int db, int qb) { return 16 * (2 * db + qb); }
constexpr int AQ_(int qb, int d0) { return 128 + 32 * qb + 4 * d0; }
constexpr int AK_(int kb, int d0) { return 192 + 32 * kb + 4 * d0; }

template <int I> DI void tr_read(W4C& c, unsigned vslot) {
  constexpr int ks = I >> 3, db = (I >> 1) & 3;
  if constexpr ((I & 1) == 0) asm volatile("ds_read_b64_tr_b16 %0, %1 offset:%2" : "=v"(c.vlo[db][ks]) : "v"(c.vvb + vslot), "i"(4096 * ks + 512 * db) : "memory");
  else                        asm volatile("ds_read_b64_tr_b16 %0, %1 offset:%2" : "=v"(c.vup[db][ks]) : "v"(c.vvb + vslot), "i"(4096 * ks + 2048 + 512 * db) : "memory");
}
template <int I> DI void k_read(W4C& c, unsigned slot) {
  constexpr int kb = I >> 3, d0 = I & 7;
  asm volatile("ds_read_b128 a[%c1:%c2], %0 offset:%c3" :: "v"(c.vkb[d0 & 3] + slot), "i"(AK_(kb, d0)), "i"(AK_(kb, d0) + 3), "i"(8192 * kb + 128 * (d0 >> 2)) : "memory", W4_ACLOB_HI);
}
template <int X, bool FIRST, int M> DI void qk_mfma(W4C& c) {
  constexpr int d0 = M >> 2, kb = (M >> 1) & 1, qb = M & 1, ak = AK_(kb, d0), aq = AQ_(qb, d0);
  if constexpr (d0 == 0) {
    if constexpr (FIRST) asm volatile("v_mfma_f32_32x32x16_bf16 %0, a[%c1:%c2], a[%c3:%c4], 0" : "=&v"(c.S[X][kb][qb]) : "i"(ak), "i"(ak + 3), "i"(aq), "i"(aq + 3) : W4_ACLOB, W4_ACLOB_HI);
    else                 asm volatile("v_mfma_f32_32x32x16_bf16 %0, a[%c2:%c3], a[%c4:%c5], %1" : "=&v"(c.S[X][kb][qb]) : "v"(c.NEGM[qb]), "i"(ak), "i"(ak + 3), "i"(aq), "i"(aq + 3) : W4_ACLOB, W4_ACLOB_HI);
  } else asm volatile("v_mfma_f32_32x32x16_bf16 %0, a[%c1:%c2], a[%c3:%c4], %0" : "+v"(c.S[X][kb][qb]) : "i"(ak), "i"(ak + 3), "i"(aq), "i"(aq + 3) : W4_ACLOB, W4_ACLOB_HI);
}
template <int M, bool ZERO> DI void pv_mfma(W4C& c) {
  constexpr int ks = M >> 3, db = (M >> 1) & 3, qb = M & 1, ao = AO_(db, qb);
  if constexpr (ZERO && ks == 0) asm volatile("v_mfma_f32_32x32x16_bf16 a[%c2:%c3], %0, %1, 0" :: "v"(c.Vf[db][ks]), "v"(c.Pk[qb][ks]), "i"(ao), "i"(ao + 15) : W4_ACLOB);
  else                         asm volatile("v_mfma_f32_32x32x16_bf16 a[%c2:%c3], %0, %1, a[%c2:%c3]" :: "v"(c.Vf[db][ks]), "v"(c.Pk[qb][ks]), "i"(ao), "i"(ao + 15) : W4_ACLOB);
}
constexpr int E_kb(int e) { return e >> 5; }
constexpr int E_q(int e)  { return (e >> 4) & 1; }
constexpr int E_r(int e)  { return e & 15; }
template <int X, int E> DI void exp1(W4C& c) { c.S[X][E_kb(E)][E_q(E)][E_r(E)] = __builtin_amdgcn_exp2f(c.S[X][E_kb(E)][E_q(E)][E_r(E)]); }
template <int X, int E> DI void add1(W4C& c) { c.ls[E_q(E)][E & 1] = fadd_s(c.ls[E_q(E)][E & 1], c.S[X][E_kb(E)][E_q(E)][E_r(E)]); }
template <int X, int P> DI void cvt1(W4C& c) {
  constexpr int e = 2 * P, kb = E_kb(e), q = E_q(e), r = E_r(e), ks = 2 * kb + (r >> 3), d = (r & 7) >> 1;
  u32x4 w = __builtin_bit_cast(u32x4, c.Pk[q][ks]);
  w[d] = cvtpk(c.S[X][kb][q][r], c.S[X][kb][q][r + 1]);
  c.Pk[q][ks] = __builtin_bit_cast(bf16x8, w);
  asm volatile("" :: "v"(w[d]));
}
template <bool FIRST> DI void max_group(float& m0, float& m1, const f32x16& s0, const f32x16& s1, const int r) {
  if constexpr (FIRST) asm("v_max3_f32 %0, %2, %3, %6\n\tv_max3_f32 %1, %4, %5, %7\n\tv_max3_f32 %0, %0, %8, %9" : "=&v"(m0), "=&v"(m1)
                           : "v"(s0[r]), "v"(s0[r + 1]), "v"(s0[r + 2]), "v"(s0[r + 3]), "v"(s1[r]), "v"(s1[r + 1]), "v"(s1[r + 2]), "v"(s1[r + 3]));
  else asm("v_max3_f32 %0, %0, %2, %3\n\tv_max3_f32 %1, %1, %4, %5\n\tv_max3_f32 %0, %0, %6, %7\n\tv_max3_f32 %1, %1, %8, %9" : "+v"(m0), "+v"(m1)
           : "v"(s0[r]), "v"(s0[r + 1]), "v"(s0[r + 2]), "v"(s0[r + 3]), "v"(s1[r]), "v"(s1[r + 1]), "v"(s1[r + 2]), "v"(s1[r + 3]));
}
#define SET_M0(V) asm volatile("s_mov_b32 m0, %0" :: "s"(V) : "m0", "memory")
#define DMA_OFF_NT(VOFFV, SRD, SOFF, OFF) asm volatile("buffer_load_dwordx4 %0, %1, %2 offen offset:%3 nt lds" :: "v"(VOFFV), "s"(SRD), "s"(SOFF), "i"(OFF) : "memory")

template <int X, bool DMA, int M> DI void gapA(W4C& c, unsigned vslot, unsigned dm0, unsigned dso) {
  constexpr int Xp = X ^ 1;
  if constexpr (DMA && M >= 24 && (M & 1) == 0) SET_M0(dm0);
  qk_mfma<X, false, M>(c);
  GAP();
  if constexpr (DMA && M >= 24 && (M & 1) == 0) { constexpr int q = (M - 24) >> 1; DMA_OFF_NT(c.vvsrc2[q & 1], c.srdV, dso, 1024 * q); }
  if constexpr (M == 8) asm volatile("" :: "v"(c.NEGM[0]), "v"(c.NEGM[1]));
  if constexpr (M < 12) { tr_read<M>(c, vslot); exp1<Xp, 40 + 2 * M>(c); cvt1<Xp, M>(c); exp1<Xp, 41 + 2 * M>(c); }
  else if constexpr (M < 22) { constexpr int a0 = 18 + 3 * (M - 12);
    tr_read<12 + 2 * (M - 12)>(c, vslot); add1<Xp, a0>(c); add1<Xp, a0 + 1>(c); cvt1<Xp, M>(c); tr_read<13 + 2 * (M - 12)>(c, vslot); add1<Xp, a0 + 2>(c); }
  else { cvt1<Xp, M>(c); if constexpr (M < 30) { add1<Xp, 48 + 2 * (M - 22)>(c); add1<Xp, 49 + 2 * (M - 22)>(c); } }
  GAP();
}
#define W4_VLO "+v"(c.vlo[0][0]), "+v"(c.vlo[0][1]), "+v"(c.vlo[0][2]), "+v"(c.vlo[0][3]), "+v"(c.vlo[1][0]), "+v"(c.vlo[1][1]), "+v"(c.vlo[1][2]), "+v"(c.vlo[1][3]), \
               "+v"(c.vlo[2][0]), "+v"(c.vlo[2][1]), "+v"(c.vlo[2][2]), "+v"(c.vlo[2][3]), "+v"(c.vlo[3][0]), "+v"(c.vlo[3][1]), "+v"(c.vlo[3][2]), "+v"(c.vlo[3][3])
#define W4_VUP "+v"(c.vup[0][0]), "+v"(c.vup[0][1]), "+v"(c.vup[0][2]), "+v"(c.vup[0][3]), "+v"(c.vup[1][0]), "+v"(c.vup[1][1]), "+v"(c.vup[1][2]), "+v"(c.vup[1][3]), \
               "+v"(c.vup[2][0]), "+v"(c.vup[2][1]), "+v"(c.vup[2][2]), "+v"(c.vup[2][3]), "+v"(c.vup[3][0]), "+v"(c.vup[3][1]), "+v"(c.vup[3][2]), "+v"(c.vup[3][3])
template <int X, bool DMA, int VM, int... Ms> DI void phaseA_(W4C& c, unsigned sl_m1, unsigned sl_p2, unsigned tb, iseq<Ms...>) {
  const unsigned dm0 = c.m0v + sl_p2, dso = tb + c.sofv2;
  (gapA<X, DMA, Ms>(c, sl_m1, dm0, dso), ...);
  asm volatile("s_waitcnt vmcnt(%16) lgkmcnt(0)" : W4_VLO : "i"(VM) : "memory"); asm volatile("s_barrier" : W4_VUP :: "memory");
  GAP();
  #pragma unroll
  for (int ks = 0; ks < 4; ++ks)
    #pragma unroll
    for (int db = 0; db < 4; ++db) c.Vf[db][ks] = __builtin_shufflevector(c.vlo[db][ks], c.vup[db][ks], 0, 1, 2, 3, 4, 5, 6, 7);
}
template <int X, bool DMA, int VM> DI void phaseA(W4C& c, unsigned sl_m1, unsigned sl_p2, unsigned tb) { phaseA_<X, DMA, VM>(c, sl_m1, sl_p2, tb, make_iseq<32>{}); }

template <int X, bool PEEL, bool ZERO1, bool DMA, bool KR, int M> DI void gapB(W4C& c, const unsigned kslot_rd, unsigned dm0, unsigned dso) {
  if constexpr (DMA && (M == 0 || M == 12 || M == 13)) SET_M0(dm0);
  if constexpr (!PEEL) pv_mfma<M, ZERO1>(c); else asm volatile("s_nop 0");
  GAP();
  if constexpr (DMA && M == 0) { DMA_OFF_NT(c.vksrc[0], c.srdK, dso, 0); DMA_OFF_NT(c.vksrc[1], c.srdK, dso, 1024); }
  if constexpr (DMA && M == 12) DMA_OFF_NT(c.vksrc[0], c.srdK, dso, 2048);
  if constexpr (DMA && M == 13) DMA_OFF_NT(c.vksrc[1], c.srdK, dso, 3072);
  if constexpr (M >= 1 && M <= 8) {
    constexpr int i = M - 1, q = i >> 2, r = 4 * (i & 3);
    if constexpr (KR) k_read<2 * i>(c, kslot_rd);
    max_group<(i & 3) == 0>(c.pmx0[q], c.pmx1[q], c.S[X][0][q], c.S[X][1][q], r);
    if constexpr (KR) k_read<2 * i + 1>(c, kslot_rd);
  }
  if constexpr (M == 9 || M == 10) {
    constexpr int q = M - 9; const float mm = max2f(c.pmx0[q], c.pmx1[q]);
    auto pr = __builtin_amdgcn_permlane32_swap(__float_as_uint(mm), __float_as_uint(mm), false, false);
    c.rmax[q] = max2f(__uint_as_float(pr[0]), __uint_as_float(pr[1]));
  }
  if constexpr (M == 11) {
    if constexpr (PEEL) {
      #pragma unroll
      for (int q = 0; q < 2; ++q) { c.mhat[q] = c.rmax[q];
        #pragma unroll
        for (int kb = 0; kb < 2; ++kb) { _Pragma("unroll") for (int e = 0; e < 16; ++e) c.S[X][kb][q][e] = fsub_s(c.S[X][kb][q][e], c.rmax[q]); }
        negm_bcast(c.NEGM[q], w4_aone(), -c.mhat[q]);
      }
    } else {
      const bool grew = max2f(c.rmax[0], c.rmax[1]) > THR;
      if (__builtin_expect(__any((int)grew), 0)) {
        #pragma unroll
        for (int q = 0; q < 2; ++q) { const float dl = max2f(c.rmax[q], 0.f); c.mhat[q] = fadd_s(c.mhat[q], dl); c.fct[q] = __builtin_amdgcn_exp2f(-dl);
          #pragma unroll
          for (int kb = 0; kb < 2; ++kb) { _Pragma("unroll") for (int e = 0; e < 16; ++e) c.S[X][kb][q][e] = fsub_s(c.S[X][kb][q][e], dl); }
          negm_bcast(c.NEGM[q], w4_aone(), -c.mhat[q]);
          asm volatile("s_nop 0" : "+v"(c.fct[q]));
          c.ls[q][0] = fmul_s(c.ls[q][0], c.fct[q]); c.ls[q][1] = fmul_s(c.ls[q][1], c.fct[q]);
        }
        c.pend = true;
      }
    }
  }
  if constexpr (M >= 12) {
    exp1<X, 2 * (M - 12)>(c);
    if constexpr (M >= 14) add1<X, M - 14>(c);
    exp1<X, 2 * (M - 12) + 1>(c);
  }
  if constexpr (M == 31) asm volatile("" :: "v"(c.S[X][0][0]), "v"(c.S[X][0][1]), "v"(c.S[X][1][0]), "v"(c.NEGM[0]), "v"(c.NEGM[1]), "v"(c.ls[0][0]), "v"(c.ls[0][1]), "v"(c.ls[1][0]), "v"(c.ls[1][1]));
  GAP();
}
template <int X, bool PEEL, bool ZERO1, bool DMA, bool KR, int... Ms> DI void phaseB_(W4C& c, unsigned sl_p1, unsigned sl_p3, unsigned tb, iseq<Ms...>) {
  const unsigned dm0 = c.m0k + sl_p3, dso = tb + c.sofk3;
  if constexpr (!PEEL) asm volatile("s_nop 1" ::: "memory");
  GAP();
  (gapB<X, PEEL, ZERO1, DMA, KR, Ms>(c, sl_p1, dm0, dso), ...);
  if constexpr (!PEEL) {
    if (__builtin_expect(c.pend, 0)) {
      float t0, t1;
      asm volatile("s_nop 7\n\ts_nop 7\n\t" W4_RESCALE_TEXT "s_nop 1" : "=&v"(t0), "=&v"(t1) : "v"(c.fct[0]), "v"(c.fct[1]) : W4_ACLOB);
      c.pend = false;
    }
  }
  asm volatile("s_waitcnt lgkmcnt(0)" ::: "memory");
  GAP();
}
template <int X, bool PEEL, bool ZERO1, bool DMA, bool KR> DI void phaseB(W4C& c, unsigned sl_p1, unsigned sl_p3, unsigned tb) { phaseB_<X, PEEL, ZERO1, DMA, KR>(c, sl_p1, sl_p3, tb, make_iseq<32>{}); }

template <int... Is> DI void tr_reads(W4C& c, unsigned vslot, iseq<Is...>) { (tr_read<Is>(c, vslot), ...); }
template <int X, int... Es> DI void exps_from40(W4C& c, iseq<Es...>) { (exp1<X, 40 + Es>(c), ...); }
template <int X, int... Ps> DI void cvts(W4C& c, iseq<Ps...>) { (cvt1<X, Ps>(c), ...); }
template <int X, int... Es> DI void adds_from18(W4C& c, iseq<Es...>) { (add1<X, 18 + Es>(c), ...); }

struct QC {
  unsigned vxb[4], vwb, vxsrc[2], vwsrc, m0x, m0w, sofx, sofw;
  __amdgpu_buffer_rsrc_t srdX, srdW, srdO;
  unsigned vwo, sofo, wo[32];
  bf16x4 alo[2][4], aup[2][4];
  bf16x8 xb[2][2];
  f32x16 acc[4][2];
};
#define DMA_ASM(M0V, VOFFV, SRD, SOFF) asm volatile("s_mov_b32 m0, %0\n\ts_nop 0\n\tbuffer_load_dwordx4 %1, %2, %3 offen lds" :: "s"(M0V), "v"(VOFFV), "s"(SRD), "s"(SOFF) : "m0", "memory")
#define DMA_ASM_NT(M0V, VOFFV, SRD, SOFF) asm volatile("s_mov_b32 m0, %0\n\ts_nop 0\n\tbuffer_load_dwordx4 %1, %2, %3 offen nt lds" :: "s"(M0V), "v"(VOFFV), "s"(SRD), "s"(SOFF) : "m0", "memory")
template <int CI, int P> DI void q_dma(QC& q) {
  if constexpr (P < 8) DMA_ASM(q.m0x + QBUF(CI) + 1024u * P, q.vxsrc[P & 1], q.srdX, q.sofx + 8192u * P + 128u * CI);
  else { constexpr int pp = P - 8; DMA_ASM(q.m0w + QBUF(CI) + 1024u * pp, q.vwsrc, q.srdW, q.sofw + 524288u * CI + 65536u * (pp >> 1) + 128u * (pp & 1)); }
}
template <int T, int P> DI void kv_dma(W4C& c) {
  if constexpr (P < 4) DMA_ASM_NT(c.m0k + tslot(T) + 1024u * P, c.vksrc[P & 1], c.srdK, c.sofk + 16384u * T + 1024u * P);
  else { constexpr int pp = P - 4; DMA_ASM_NT(c.m0v + tslot(T) + 1024u * pp, c.vvsrc, c.srdV, c.sofv + 16384u * T + 2048u * (pp >> 1) + 128u * (pp & 1)); }
}
template <int CI, int KS, int R> DI void q_read(QC& q) {
  constexpr int PAR = KS & 1;
  if constexpr (R < 8) {
    constexpr int db = R >> 1;
    if constexpr ((R & 1) == 0) asm volatile("ds_read_b64_tr_b16 %0, %1 offset:%2" : "=v"(q.alo[PAR][db]) : "v"(q.vwb + QBUF(CI)), "i"(4096 * KS + 512 * db) : "memory");
    else                        asm volatile("ds_read_b64_tr_b16 %0, %1 offset:%2" : "=v"(q.aup[PAR][db]) : "v"(q.vwb + QBUF(CI)), "i"(4096 * KS + 512 * db + 256) : "memory");
  } else {
    constexpr int qb = R - 8;
    asm volatile("ds_read_b128 %0, %1 offset:%2" : "=v"(q.xb[PAR][qb]) : "v"(q.vxb[KS] + QBUF(CI)), "i"(4096 * qb) : "memory");
  }
}
template <int CI, int KS, int M> DI void q_mfma(QC& q) {
  constexpr int PAR = KS & 1, db = M >> 1, qb = M & 1;
  const bf16x8 a = __builtin_shufflevector(q.alo[PAR][db], q.aup[PAR][db], 0, 1, 2, 3, 4, 5, 6, 7);
  if constexpr (CI == 0 && KS == 0) asm volatile("v_mfma_f32_32x32x16_bf16 %0, %1, %2, 0" : "=&v"(q.acc[db][qb]) : "v"(a), "v"(q.xb[PAR][qb]));
  else                              asm volatile("v_mfma_f32_32x32x16_bf16 %0, %1, %2, %0" : "+v"(q.acc[db][qb]) : "v"(a), "v"(q.xb[PAR][qb]));
}

constexpr int sched_dma(int ci, int ks, int m) {
  const int j = m == 1 ? 0 : (m == 4 ? 1 : (m == 7 ? 2 : -1));
  if (ci == 7 && ks == 3) return (m & 1) ? 1000 + 16 + (m >> 1) : -1;
  if (ci == 0 && ks < 3) return (m & 1) ? 200 + 4 * ks + (m >> 1) : (m == 2 ? 1000 + ks : ((m == 6 && ks == 2) ? 1003 : -1));
  if (j < 0) return -1;
  const int i = ks == 3 ? ci : ci - 1, st = ks == 3 ? 0 : ks + 1, p = 3 * st + j;
  if (i < 0) return -1;
  if (i + 3 < 8) return 100 * (i + 3) + p;
  if (i == 5) return p < 4 ? 1000 + 4 + p : -1;
  if (i == 6) return p < 8 ? 1000 + 8 + p : -1;
  return -1;
}
constexpr int sched_wo(int ci, int ks, int m) { return (ci >= 1 && ci <= 4 && (m == 2 || m == 5)) ? ((ci - 1) * 4 + ks) * 2 + (m == 5) : -1; }
constexpr int sched_vm(int ci) {
  int n = 0;
  for (int c = 0; c < 2; ++c) for (int p = 0; p < 12; ++p) { n = (c == ci + 1) ? 0 : n + 1; }
  for (int c = 0; c <= ci; ++c) for (int ks = 0; ks < 4; ++ks) { if (c == ci && ks == 3) break;
    for (int m = 0; m < 8; ++m) {
      const int d = sched_dma(c, ks, m);
      if (d >= 0) n = ((d < 1000 && d / 100 == ci + 1) || (ci == 7 && d >= 1000 && d < 1004)) ? 0 : n + 1;
      if (sched_wo(c, ks, m) >= 0) n = (ci == 7) ? 0 : n + 1;
    } }
  return n;
}
template <int I> DI void wo_load(QC& q) {
  constexpr int i = I >> 3, j = I & 7;
  asm volatile("buffer_load_ushort %0, %1, %2, %3 offen offset:%4" : "=v"(q.wo[I]) : "v"(q.vwo), "s"(q.srdO), "s"(q.sofo + 8192u * (j >> 2)), "i"(1024 * (j & 3) + 64 * i) : "memory");
}
template <int CI, int KS, int M> DI void q_gap(QC& q, W4C& c) {
  q_mfma<CI, KS, M>(q);
  constexpr int NCI = KS == 3 ? CI + 1 : CI, NKS = (KS + 1) & 3;
  if constexpr (NCI < 8 && M < 5) { q_read<NCI, NKS, 2 * M>(q); q_read<NCI, NKS, 2 * M + 1>(q); }
  constexpr int D = sched_dma(CI, KS, M), W = sched_wo(CI, KS, M);
  if constexpr (D >= 1000) kv_dma<((D - 1000) >> 3), ((D - 1000) & 7)>(c);
  else if constexpr (D >= 0) q_dma<(D / 100), (D % 100)>(q);
  if constexpr (W >= 0) wo_load<W>(q);
  if constexpr (KS == 3 && CI == 7) { k_read<2 * M>(c, tslot(0)); k_read<2 * M + 1>(c, tslot(0)); }
  GAP();
}
template <int CI, int KS, int... Ms> DI void q_step_(QC& q, W4C& c, iseq<Ms...>) {
  constexpr int PAR = KS & 1;
  if constexpr (KS == 3) {
    constexpr int VM = sched_vm(CI);
    asm volatile("s_waitcnt vmcnt(%10) lgkmcnt(0)\n\ts_barrier" : "+v"(q.alo[PAR][0]), "+v"(q.alo[PAR][1]), "+v"(q.alo[PAR][2]), "+v"(q.alo[PAR][3]),
                 "+v"(q.aup[PAR][0]), "+v"(q.aup[PAR][1]), "+v"(q.aup[PAR][2]), "+v"(q.aup[PAR][3]), "+v"(q.xb[PAR][0]), "+v"(q.xb[PAR][1]) : "i"(VM) : "memory");
  } else {
    asm volatile("s_waitcnt lgkmcnt(0)" : "+v"(q.alo[PAR][0]), "+v"(q.alo[PAR][1]), "+v"(q.alo[PAR][2]), "+v"(q.alo[PAR][3]),
                 "+v"(q.aup[PAR][0]), "+v"(q.aup[PAR][1]), "+v"(q.aup[PAR][2]), "+v"(q.aup[PAR][3]), "+v"(q.xb[PAR][0]), "+v"(q.xb[PAR][1]) :: "memory");
  }
  GAP();
  (q_gap<CI, KS, Ms>(q, c), ...);
}
template <int CI> DI void q_chunk(QC& q, W4C& c) {
  q_step_<CI, 0>(q, c, make_iseq<8>{}); q_step_<CI, 1>(q, c, make_iseq<8>{}); q_step_<CI, 2>(q, c, make_iseq<8>{}); q_step_<CI, 3>(q, c, make_iseq<8>{});
}
template <int... Cs> DI void q_chunks(QC& q, W4C& c, iseq<Cs...>) { (q_chunk<Cs>(q, c), ...); }
template <int CI, int... Ps> DI void q_dmas(QC& q, iseq<Ps...>) { (q_dma<CI, Ps>(q), ...); }
template <int... Rs> DI void q_reads0(QC& q, iseq<Rs...>) { (q_read<0, 0, Rs>(q), ...); }

template <int BASE> DI void acc_read8(float (&o)[8]) {
  asm volatile("v_accvgpr_read_b32 %0, a[%c8]\n\tv_accvgpr_read_b32 %1, a[%c9]\n\tv_accvgpr_read_b32 %2, a[%c10]\n\tv_accvgpr_read_b32 %3, a[%c11]\n\t"
               "v_accvgpr_read_b32 %4, a[%c12]\n\tv_accvgpr_read_b32 %5, a[%c13]\n\tv_accvgpr_read_b32 %6, a[%c14]\n\tv_accvgpr_read_b32 %7, a[%c15]"
               : "=v"(o[0]), "=v"(o[1]), "=v"(o[2]), "=v"(o[3]), "=v"(o[4]), "=v"(o[5]), "=v"(o[6]), "=v"(o[7])
               : "i"(BASE), "i"(BASE + 1), "i"(BASE + 2), "i"(BASE + 3), "i"(BASE + 4), "i"(BASE + 5), "i"(BASE + 6), "i"(BASE + 7));
}
template <int AQ> DI void acc_write4(const u32x4& w) {
  asm volatile("v_accvgpr_write_b32 a[%c4], %0\n\tv_accvgpr_write_b32 a[%c5], %1\n\tv_accvgpr_write_b32 a[%c6], %2\n\tv_accvgpr_write_b32 a[%c7], %3"
               :: "v"(w[0]), "v"(w[1]), "v"(w[2]), "v"(w[3]), "i"(AQ), "i"(AQ + 1), "i"(AQ + 2), "i"(AQ + 3) : W4_ACLOB_HI);
}
template <int I> DI void q_convert(QC& q, float C) {
  constexpr int qb = I & 1, d0 = I >> 1, db = d0 >> 1, s = d0 & 1;
  u32x4 w;
  #pragma unroll
  for (int e = 0; e < 4; ++e) w[e] = cvtpk(fmul_s(q.acc[db][qb][8 * s + 2 * e], C), fmul_s(q.acc[db][qb][8 * s + 2 * e + 1], C));
  acc_write4<AQ_(qb, d0)>(w);
}
template <int M> DI void gapA0(W4C& c, QC& q, float C) {
  qk_mfma<0, true, M>(c);
  GAP();
  if constexpr (M < 28 && (M & 3) < 2) { q_convert<2 * ((M >> 2) + 1) + (M & 3)>(q, C); asm volatile("s_nop 1" ::: "memory"); }
  if constexpr (M >= 24 && (M & 1) == 0) kv_dma<2, 4 + ((M - 24) >> 1)>(c);
  GAP();
}
template <int... Ms> DI void phaseA0(W4C& c, QC& q, float C, iseq<Ms...>) {
  (gapA0<Ms>(c, q, C), ...);
  asm volatile("s_waitcnt vmcnt(16) lgkmcnt(0)\n\ts_barrier" ::: "memory");
  GAP();
}
template <int I> DI void o_store(const float (&il)[2], char* obase, unsigned voff) {
  constexpr int qb = I >> 3, kk = I & 7, db = kk >> 1, s = kk & 1;
  float o[8]; acc_read8<AO_(db, qb) + 8 * s>(o);
  u32x4 w;
  #pragma unroll
  for (int e = 0; e < 4; ++e) w[e] = cvtpk(fmul_s(o[2 * e], il[qb]), fmul_s(o[2 * e + 1], il[qb]));
  __builtin_amdgcn_raw_buffer_store_b128(w, __builtin_amdgcn_make_buffer_rsrc((void*)obase, (short)0, 512 * 1024, SRD_WORD3), voff + kk * 1024, qb * (256 * 1024), ST_AUX);
}
template <int J, int KS> DI void pv_last(W4C& c) {
  constexpr int db = J >> 1, qb = J & 1, ao = AO_(db, qb);
  asm volatile("v_mfma_f32_32x32x16_bf16 a[%c2:%c3], %0, %1, a[%c2:%c3]" :: "v"(c.Vf[db][KS]), "v"(c.Pk[qb][KS]), "i"(ao), "i"(ao + 15) : W4_ACLOB);
}
template <int J> DI void drain_acc(W4C& c, const float (&il)[2], char* obase, unsigned voff) {
  constexpr int Jp = J - 1;
  pv_last<J, 0>(c); GAP();
  pv_last<J, 1>(c); GAP();
  pv_last<J, 2>(c); GAP();
  if constexpr (J > 0) { asm volatile("s_nop 7\n\ts_nop 1" ::: "memory"); o_store<8 * (Jp & 1) + 2 * (Jp >> 1)>(il, obase, voff); }
  GAP();
  pv_last<J, 3>(c); GAP();
  if constexpr (J > 0) o_store<8 * (Jp & 1) + 2 * (Jp >> 1) + 1>(il, obase, voff);
  GAP();
}
template <int... Js> DI void drain_accs(W4C& c, const float (&il)[2], char* obase, unsigned voff, iseq<Js...>) { (drain_acc<Js>(c, il, obase, voff), ...); }

constexpr size_t WS_OF = 0, WS_WOF = (size_t)16 << 20;

extern "C" __global__ __launch_bounds__(256, 1)
void attn_fused(const unsigned short* __restrict__ X, const unsigned short* __restrict__ Kc, const unsigned short* __restrict__ Vc,
                const unsigned short* __restrict__ Wq, const unsigned short* __restrict__ Wo, char* __restrict__ ws) {
  extern __shared__ __attribute__((aligned(16))) char lds[];
  const unsigned tid = threadIdx.x, lane = tid & 63u, wid = tid >> 6, l32 = lane & 31u, hi = lane >> 5;
  const unsigned g = blockIdx.x, hd = 4u * (g & 7u) + ((g >> 3) & 3u), b = g >> 5;
  const float C = 0.12751743082f;
  W4C c; QC q;
  c.ldsb = (unsigned)(size_t)lds;
  #pragma unroll
  for (int j = 0; j < 4; ++j) c.vkb[j] = c.ldsb + KOFF + 256u * l32 + 16u * ((2u * j + hi) ^ (l32 & 7u));
  c.vvb = c.ldsb + (((lane & 3u) << 3) | (((lane >> 2) & 3u) << 6) | (((lane >> 4) & 1u) << 5) | (hi << 8));
  #pragma unroll
  for (int par = 0; par < 2; ++par) c.vksrc[par] = 256u * (lane >> 4) + 16u * ((lane & 15u) ^ (4u * par + (lane >> 4)));
  c.vvsrc = 256u * ((lane >> 2) & 7u) + 64u * hi + 16u * (lane & 3u);
  c.m0k = rfl(c.ldsb + KOFF + 4096u * wid); c.m0v = rfl(c.ldsb + 4096u * wid);
  c.sofk = rfl(4096u * wid); c.sofv = rfl(4096u * wid);
  c.vvsrc2[0] = c.vvsrc + 896u; c.vvsrc2[1] = c.vvsrc; c.sofv2 = c.sofv + 32768u - 896u; c.sofk3 = c.sofk + 49152u;
  const size_t kvoff = ((size_t)b * NH + hd) * (SK * DH * 2);
  c.srdK = __builtin_amdgcn_make_buffer_rsrc((void*)((const char*)Kc + kvoff), (short)0, SK * DH * 2, SRD_WORD3);
  c.srdV = __builtin_amdgcn_make_buffer_rsrc((void*)((const char*)Vc + kvoff), (short)0, SK * DH * 2, SRD_WORD3);
  #pragma unroll
  for (int ks = 0; ks < 4; ++ks) q.vxb[ks] = c.ldsb + 8192u * wid + 128u * l32 + 16u * ((2u * ks + hi) ^ ((l32 >> 1) & 7u));
  { const unsigned p = lane & 3u, sp = ((p & 1u) << 1) | (p >> 1);
    q.vwb = c.ldsb + QWOFF + ((hi << 11) | (((lane >> 2) & 3u) << 6) | (((lane >> 4) & 1u) << 5) | (sp << 3)); }
  #pragma unroll
  for (int par = 0; par < 2; ++par) q.vxsrc[par] = 1024u * (lane >> 3) + 16u * ((lane & 7u) ^ (4u * par + (lane >> 4)));
  q.vwsrc = 8192u * ((lane >> 2) & 7u) + 64u * hi + 16u * (lane & 3u);
  q.m0x = rfl(c.ldsb + 8192u * wid); q.m0w = rfl(c.ldsb + QWOFF + 4096u * wid);
  q.sofx = rfl(65536u * wid); q.sofw = rfl(131072u * wid);
  q.srdX = __builtin_amdgcn_make_buffer_rsrc((void*)((const char*)X + (size_t)b * (SQ * DM * 2)), (short)0, SQ * DM * 2, SRD_WORD3);
  q.srdW = __builtin_amdgcn_make_buffer_rsrc((void*)((const char*)Wq + hd * 256u), (short)0, DM * NH * DH * 2 - (int)hd * 256, SRD_WORD3);
  q.srdO = __builtin_amdgcn_make_buffer_rsrc((void*)Wo, (short)0, NH * DH * DM * 2, SRD_WORD3);
  q.vwo = 4096u * hi + 2u * l32; q.sofo = rfl(16384u * g + 256u * wid);
  asm volatile("s_nop 4" ::: "memory");
  q_dmas<0>(q, make_iseq<12>{});
  q_dmas<1>(q, make_iseq<12>{});
  asm volatile("s_waitcnt vmcnt(12)\n\ts_barrier" ::: "memory");
  q_reads0(q, make_iseq<10>{});
  q_chunks(q, c, make_iseq<8>{});
  asm volatile("" : "+v"(q.wo[0]), "+v"(q.wo[1]), "+v"(q.wo[2]), "+v"(q.wo[3]), "+v"(q.wo[4]), "+v"(q.wo[5]), "+v"(q.wo[6]), "+v"(q.wo[7]),
                    "+v"(q.wo[8]), "+v"(q.wo[9]), "+v"(q.wo[10]), "+v"(q.wo[11]), "+v"(q.wo[12]), "+v"(q.wo[13]), "+v"(q.wo[14]), "+v"(q.wo[15]));
  asm volatile("" : "+v"(q.wo[16]), "+v"(q.wo[17]), "+v"(q.wo[18]), "+v"(q.wo[19]), "+v"(q.wo[20]), "+v"(q.wo[21]), "+v"(q.wo[22]), "+v"(q.wo[23]),
                    "+v"(q.wo[24]), "+v"(q.wo[25]), "+v"(q.wo[26]), "+v"(q.wo[27]), "+v"(q.wo[28]), "+v"(q.wo[29]), "+v"(q.wo[30]), "+v"(q.wo[31]));
  #pragma unroll
  for (int i = 0; i < 4; ++i) {
    u32x4 w;
    #pragma unroll
    for (int e = 0; e < 4; ++e) w[e] = q.wo[8 * i + 2 * e] | (q.wo[8 * i + 2 * e + 1] << 16);
    __builtin_amdgcn_raw_buffer_store_b128(w, __builtin_amdgcn_make_buffer_rsrc((void*)(ws + WS_WOF), (short)0, 4 << 20, SRD_WORD3), lane * 16u, rfl(((4u * wid + i) * 256u + g) * 1024u), ST_AUX);
  }
  asm volatile("s_nop 7\n\ts_nop 7" : "+v"(q.acc[0][0]), "+v"(q.acc[0][1]), "+v"(q.acc[1][0]), "+v"(q.acc[1][1]), "+v"(q.acc[2][0]), "+v"(q.acc[2][1]), "+v"(q.acc[3][0]), "+v"(q.acc[3][1]) :: "memory");
  q_convert<0>(q, C); q_convert<1>(q, C);
  asm volatile("s_waitcnt lgkmcnt(0)\n\ts_nop 1" ::: "memory");
  c.mhat[0] = c.mhat[1] = 0.f; c.ls[0][0] = c.ls[0][1] = c.ls[1][0] = c.ls[1][1] = 0.f; c.fct[0] = c.fct[1] = 1.f; c.pend = false;
  phaseA0(c, q, C, make_iseq<32>{});                                   phaseB<0, true,  false, true, true>(c, tslot(1), tslot(3), 0u);
  phaseA<1, true, 16>(c, tslot(0), tslot(3), 16384u);                  phaseB<1, false, true,  true, true>(c, tslot(2), tslot(4), 16384u);
  {
    unsigned s0 = tslot(1), s1 = tslot(2), s2 = tslot(3), s3 = tslot(4), s4 = tslot(5), tb = 32768u;
    for (int t = 2; t < 16; t += 2) {
      phaseA<0, true, 12>(c, s0, s3, tb);          phaseB<0, false, false, true, true>(c, s2, s4, tb);
      phaseA<1, true, 12>(c, s1, s4, tb + 16384u); phaseB<1, false, false, true, true>(c, s3, s0, tb + 16384u);
      const unsigned n0 = s2, n1 = s3, n2 = s4, n3 = s0, n4 = s1; s0 = n0; s1 = n1; s2 = n2; s3 = n3; s4 = n4; tb += 32768u;
    }
  }
  {
    const unsigned vslot = tslot(NT - 1);
    tr_reads(c, vslot, make_iseq<32>{});
    exps_from40<1>(c, make_iseq<24>{});
    cvts<1>(c, make_iseq<32>{});
    adds_from18<1>(c, make_iseq<46>{});
    float lsum[2];
    #pragma unroll
    for (int qb = 0; qb < 2; ++qb) { const float rs = fadd_s(c.ls[qb][0], c.ls[qb][1]);
      auto pr = __builtin_amdgcn_permlane32_swap(__float_as_uint(rs), __float_as_uint(rs), false, false);
      lsum[qb] = fadd_s(__uint_as_float(pr[0]), __uint_as_float(pr[1])); }
    asm volatile("s_waitcnt lgkmcnt(0)" : W4_VLO :: "memory"); asm volatile("" : W4_VUP :: "memory");
    GAP();
    #pragma unroll
    for (int ks = 0; ks < 4; ++ks)
      #pragma unroll
      for (int db = 0; db < 4; ++db) c.Vf[db][ks] = __builtin_shufflevector(c.vlo[db][ks], c.vup[db][ks], 0, 1, 2, 3, 4, 5, 6, 7);
    asm volatile("s_nop 1" ::: "memory");
    float il[2];
    il[0] = __builtin_amdgcn_rcpf(lsum[0]); il[1] = __builtin_amdgcn_rcpf(lsum[1]);
    asm volatile("s_nop 1" : "+v"(il[0]), "+v"(il[1]));
    char* obase = ws + WS_OF + (size_t)rfl(((b * 8u + 2u * wid) * 256u + hd * 8u) * 1024u);
    drain_accs(c, il, obase, lane * 16u, make_iseq<8>{});
    asm volatile("s_nop 7\n\ts_nop 7" ::: "memory");
    o_store<8 + 6>(il, obase, lane * 16u); o_store<8 + 7>(il, obase, lane * 16u);
  }
}

extern "C" __global__ __launch_bounds__(512, 2)
void oproj(const char* __restrict__ ws, unsigned short* __restrict__ out) {
  __shared__ __attribute__((aligned(16))) float red[8 * 64 * 68];
  const unsigned tid = threadIdx.x, lane = tid & 63u, wid = tid >> 6, l32 = lane & 31u, hi = lane >> 5;
  const unsigned g = blockIdx.x, xc = g & 7u, idx = g >> 3;
  const unsigned qt = 4u * xc + (idx & 3u), nt = idx >> 2;
  const char* ofp = ws + WS_OF + ((size_t)(2u * qt) * 256 + 32u * wid) * 1024 + lane * 16u;
  const char* wfp = ws + WS_WOF + ((size_t)(2u * nt) * 256 + 32u * wid) * 1024 + lane * 16u;
  f32x16 acc[2][2] = {};
  bf16x8 fo[4][2], fw[4][2];
  #pragma unroll
  for (int i = 0; i < 4; ++i) {
    fo[i][0] = *(const bf16x8*)(ofp + i * 1024); fo[i][1] = *(const bf16x8*)(ofp + 262144 + i * 1024);
    fw[i][0] = *(const bf16x8*)(wfp + i * 1024); fw[i][1] = *(const bf16x8*)(wfp + 262144 + i * 1024);
  }
  #pragma unroll 1
  for (int k0 = 0; k0 < 32; k0 += 4) {
    #pragma unroll
    for (int i = 0; i < 4; ++i) {
      const bf16x8 o0 = fo[i][0], o1 = fo[i][1], w0 = fw[i][0], w1 = fw[i][1];
      if (k0 + 4 < 32) {
        const int kn = k0 + 4 + i;
        fo[i][0] = *(const bf16x8*)(ofp + kn * 1024); fo[i][1] = *(const bf16x8*)(ofp + 262144 + kn * 1024);
        fw[i][0] = *(const bf16x8*)(wfp + kn * 1024); fw[i][1] = *(const bf16x8*)(wfp + 262144 + kn * 1024);
      }
      acc[0][0] = __builtin_amdgcn_mfma_f32_32x32x16_bf16(w0, o0, acc[0][0], 0, 0, 0);
      acc[0][1] = __builtin_amdgcn_mfma_f32_32x32x16_bf16(w0, o1, acc[0][1], 0, 0, 0);
      acc[1][0] = __builtin_amdgcn_mfma_f32_32x32x16_bf16(w1, o0, acc[1][0], 0, 0, 0);
      acc[1][1] = __builtin_amdgcn_mfma_f32_32x32x16_bf16(w1, o1, acc[1][1], 0, 0, 0);
    }
  }
  float* rw = red + wid * (64 * 68);
  #pragma unroll
  for (int nb = 0; nb < 2; ++nb)
    #pragma unroll
    for (int qb = 0; qb < 2; ++qb)
      #pragma unroll
      for (int gq = 0; gq < 4; ++gq) {
        f32x4 v = {acc[nb][qb][4 * gq], acc[nb][qb][4 * gq + 1], acc[nb][qb][4 * gq + 2], acc[nb][qb][4 * gq + 3]};
        *(f32x4*)(rw + (32 * qb + l32) * 68 + 32 * nb + 8 * gq + 4 * hi) = v;
      }
  __syncthreads();
  {
    const unsigned qr = tid >> 3, n0 = (tid & 7u) * 8u;
    f32x4 s0 = {}, s1 = {};
    #pragma unroll
    for (int w = 0; w < 8; ++w) {
      s0 += *(const f32x4*)(red + w * (64 * 68) + qr * 68 + n0);
      s1 += *(const f32x4*)(red + w * (64 * 68) + qr * 68 + n0 + 4);
    }
    u32x4 w; w[0] = cvtpk(s0[0], s0[1]); w[1] = cvtpk(s0[2], s0[3]); w[2] = cvtpk(s1[0], s1[1]); w[3] = cvtpk(s1[2], s1[3]);
    *(u32x4*)(out + (size_t)(64u * qt + qr) * DM + 64u * nt + n0) = w;
  }
}

extern "C" void kernel_launch(void* const* d_in, const int* in_sizes, int n_in, void* d_out, int out_size, void* d_ws, size_t ws_size, hipStream_t stream) {
  (void)in_sizes; (void)n_in; (void)out_size; (void)ws_size;
  (void)hipFuncSetAttribute((const void*)attn_fused, hipFuncAttributeMaxDynamicSharedMemorySize, (int)SHM_ATTN);
  hipLaunchKernelGGL(attn_fused, dim3(256), dim3(256), SHM_ATTN, stream, (const unsigned short*)d_in[0], (const unsigned short*)d_in[1], (const unsigned short*)d_in[2],
                     (const unsigned short*)d_in[3], (const unsigned short*)d_in[4], (char*)d_ws);
  hipLaunchKernelGGL(oproj, dim3(256), dim3(512), 0, stream, (const char*)d_ws, (unsigned short*)d_out);
}
```
